```python
import torch

def _softmax(x, axis=-1):
    return torch.softmax(x, dim=axis)
def _gelu(x):
    return torch.nn.functional.gelu(x, approximate="tanh")
def _silu(x):
    return torch.nn.functional.silu(x)
def _sigmoid(x):
    return torch.sigmoid(x)
def _layernorm(x, axis=-1, eps=1e-5):
    m = x.mean(dim=axis, keepdim=True)
    v = x.var(dim=axis, keepdim=True, unbiased=False)
    return (x - m) / torch.sqrt(v + eps)
def _scatter_add(tgt, idx, src, axis=0):
    return tgt.clone().index_add_(axis, idx, src)
def _segment_sum(src, seg, n):
    out = torch.zeros((n,) + src.shape[1:], dtype=src.dtype)
    return out.index_add_(0, seg, src)
def _pad(x, pw):
    flat = tuple(v for pair in reversed(pw) for v in pair)
    return torch.nn.functional.pad(x, flat)

kernel_name = 'parallel_sum__mha_block_cached__geglu_ffn__decode'

def setup_inputs():

    _w = (lambda a: (lambda t: t.to(torch.bfloat16) if t.is_floating_point() else t)(torch.as_tensor(a)))
    def _A_setup():
        B, Sq, D = 1, 1, 512
        H, Sk, Dh = 32, 32768, 128
        return {
            "x": _w((lambda *s: torch.randn(*s, dtype=torch.bfloat16) * 0.05)(B, Sq, D)),
            "k_cache": _w((lambda *s: torch.randn(*s, dtype=torch.bfloat16) * 3.2)(B, H, Sk, Dh)),
            "v_cache": _w((lambda *s: torch.randn(*s, dtype=torch.bfloat16) * 1.6)(B, H, Sk, Dh)),
            "wq": _w((lambda *s: torch.randn(*s, dtype=torch.bfloat16) * 3.2)(D, H * Dh) * (D ** -0.5)),
            "wo": _w((lambda *s: torch.randn(*s, dtype=torch.bfloat16) * 1.6)(H * Dh, D) * ((H * Dh) ** -0.5)),
        }
    _A_in = _A_setup()

    def _B_setup():
        B, S, D, F = 1, 1, 512, 1376
        return {
            "x": _w((lambda *s: torch.randn(*s, dtype=torch.bfloat16) * 0.05)(B, S, D)),
            "w_gate": _w((lambda *s: torch.randn(*s, dtype=torch.bfloat16) * 0.05)(D, F)),
            "w_up": _w((lambda *s: torch.randn(*s, dtype=torch.bfloat16) * 0.05)(D, F)),
            "w_down": _w((lambda *s: torch.randn(*s, dtype=torch.bfloat16) * 0.05)(F, D)),
        }
    _B_in = _B_setup()

    return {
        "A_x": _A_in["x"],
        "A_k_cache": _A_in["k_cache"],
        "A_v_cache": _A_in["v_cache"],
        "A_wq": _A_in["wq"],
        "A_wo": _A_in["wo"],
        "B_x": _B_in["x"],
        "B_w_gate": _B_in["w_gate"],
        "B_w_up": _B_in["w_up"],
        "B_w_down": _B_in["w_down"],
    }

def reference(A_x, A_k_cache, A_v_cache, A_wq, A_wo, B_x, B_w_gate, B_w_up, B_w_down):
    def _A_ref(x, k_cache, v_cache, wq, wo):
        B, Sq, D = x.shape
        H, Dh = 32, 128
        q = (lambda x, *d: x.permute(*d))((x @ wq).reshape(B, Sq, H, Dh), 0, 2, 1, 3)
        scores = torch.einsum("bhqd,bhkd->bhqk", q, k_cache) * (Dh ** -0.5)
        p = _softmax(scores, axis=-1)
        o = torch.einsum("bhqk,bhkd->bhqd", p, v_cache)
        return (lambda x, *d: x.permute(*d))(o, 0, 2, 1, 3).reshape(B, Sq, H * Dh) @ wo

    def _B_ref(x, w_gate, w_up, w_down):
        return (_gelu(x @ w_gate) * (x @ w_up)) @ w_down

    x = A_x
    xn = _layernorm(x, axis=-1)
    return x + _A_ref(xn, A_k_cache, A_v_cache, A_wq, A_wo) + _B_ref(xn, B_w_gate, B_w_up, B_w_down)
```

```cpp
#include <hip/hip_runtime.h>
#include <hip/hip_bf16.h>
#include <stdint.h>

#define PRIO_MODE 0
#define NO_FFN 0
#define MAPV 4
#define NO_COMP 0
#define TUNE_ARGS
#define TUNE_PASS
constexpr int SLOT = 3700, LEAD = -8, GAP = 0, MODE = 1;
namespace {

constexpr int D = 512, H = 32, SK = 32768, DH = 128, F = 1376;
constexpr int NWG = 256, NSPLIT = 8, WORKERS = 8, NWAVES = WORKERS + 2, NTHR = NWAVES * 64;
constexpr int KEYS_WG = SK / NSPLIT;
constexpr int TILE_KEYS = 16;
constexpr int STEPS = KEYS_WG / (WORKERS * TILE_KEYS);
constexpr int STEP_BYTES = WORKERS * TILE_KEYS * DH * 2;
constexpr int CHUNK_BYTES = KEYS_WG * DH * 2;
constexpr int FFN_UNITS = F / 8;
constexpr int FFN_PER_X = 22;
constexpr int RING = 3;
constexpr unsigned SPIN_CAP = 1u << 20;

constexpr int G_Q = 0;
constexpr int G_M = G_Q + H * 512;
constexpr int G_P = G_M + H * 16;
constexpr int YROWS = 44;
constexpr int G_Y = G_P + NWG * 128;
constexpr int G_A = G_Y + 8 * YROWS * 64;

constexpr int L_Q = 0;
constexpr int L_ML = 256;
constexpr int L_OP = 320;
constexpr int L_END = L_OP + 4096;

typedef unsigned u32x4 __attribute__((ext_vector_type(4)));
typedef float f32x2 __attribute__((ext_vector_type(2)));
typedef float f32x4 __attribute__((ext_vector_type(4)));
typedef __bf16 bf16x2 __attribute__((ext_vector_type(2)));
typedef __attribute__((address_space(1))) unsigned long long gu64;

extern "C" __device__ unsigned long long llvm_dispatch_id() __asm("llvm.amdgcn.dispatch.id");

__device__ __forceinline__ float dot2(unsigned a, unsigned b, float acc) {
  return __builtin_amdgcn_fdot2_f32_bf16(__builtin_bit_cast(bf16x2, a), __builtin_bit_cast(bf16x2, b), acc, false);
}
__device__ __forceinline__ unsigned pk_bf16(float lo, float hi) {
  f32x2 v = {lo, hi};
  return __builtin_bit_cast(unsigned, __builtin_convertvector(v, bf16x2));
}
__device__ __forceinline__ float bf_lo(unsigned x) { return __builtin_bit_cast(float, x << 16); }
__device__ __forceinline__ float bf_hi(unsigned x) { return __builtin_bit_cast(float, x & 0xffff0000u); }
__device__ __forceinline__ float rbf(float x) { return bf_lo(pk_bf16(x, x)); }

template <int CTRL> __device__ __forceinline__ unsigned dppu(unsigned x) { return (unsigned)__builtin_amdgcn_mov_dpp((int)x, CTRL, 0xf, 0xf, true); }
template <int CTRL> __device__ __forceinline__ float dpp(float x) { return __builtin_bit_cast(float, dppu<CTRL>(__builtin_bit_cast(unsigned, x))); }
constexpr int XOR1 = 0xB1, XOR2 = 0x4E, HMIR = 0x141, ROR4 = 0x124, ROR8 = 0x128;
__device__ __forceinline__ float row16_sum(float x) {
  x += dpp<XOR1>(x); x += dpp<XOR2>(x); x += dpp<HMIR>(x); x += dpp<ROR8>(x); return x;
}
__device__ __forceinline__ float oct_sum(float x) { x += dpp<XOR1>(x); x += dpp<XOR2>(x); x += dpp<HMIR>(x); return x; }
__device__ __forceinline__ float oct_max(float x) { x = fmaxf(x, dpp<XOR1>(x)); x = fmaxf(x, dpp<XOR2>(x)); x = fmaxf(x, dpp<HMIR>(x)); return x; }
__device__ __forceinline__ float swap16_add(float a, float b) {
  auto s = __builtin_amdgcn_permlane16_swap(__float_as_uint(a), __float_as_uint(b), false, false);
  return __uint_as_float(s[0]) + __uint_as_float(s[1]);
}
__device__ __forceinline__ float swap32_add(float a, float b) {
  auto s = __builtin_amdgcn_permlane32_swap(__float_as_uint(a), __float_as_uint(b), false, false);
  return __uint_as_float(s[0]) + __uint_as_float(s[1]);
}
__device__ __forceinline__ float xrow_sum(float x) { x = swap16_add(x, x); return swap32_add(x, x); }
__device__ __forceinline__ float xrow_max(float x) {
  auto s = __builtin_amdgcn_permlane16_swap(__float_as_uint(x), __float_as_uint(x), false, false);
  x = fmaxf(__uint_as_float(s[0]), __uint_as_float(s[1]));
  auto t = __builtin_amdgcn_permlane32_swap(__float_as_uint(x), __float_as_uint(x), false, false);
  return fmaxf(__uint_as_float(t[0]), __uint_as_float(t[1]));
}
__device__ __forceinline__ float wave_sum(float x) { return xrow_sum(row16_sum(x)); }
__device__ __forceinline__ float rdlane(float x, int l) { return __builtin_bit_cast(float, __builtin_amdgcn_readlane(__builtin_bit_cast(int, x), l)); }

__device__ __forceinline__ void put(gu64* g, unsigned tag, unsigned v) {
  __hip_atomic_store(g, ((unsigned long long)tag << 32) | v, __ATOMIC_RELAXED, __HIP_MEMORY_SCOPE_AGENT);
}
__device__ __forceinline__ void putf(gu64* g, unsigned tag, float v) { put(g, tag, __builtin_bit_cast(unsigned, v)); }
__device__ __forceinline__ unsigned long long get(gu64* g) { return __hip_atomic_load(g, __ATOMIC_RELAXED, __HIP_MEMORY_SCOPE_AGENT); }
template <int N, int SLEEP> __device__ __forceinline__ void sweep(gu64* g, int S, unsigned tag, unsigned (&v)[N]) {
  for (unsigned spins = 0; spins < SPIN_CAP; ++spins) {
    bool ok = true;
#pragma unroll
    for (int k = 0; k < N; ++k) { const unsigned long long x = get(g + k * S); v[k] = (unsigned)x; ok &= (unsigned)(x >> 32) == tag; }
    if (__all(ok)) return;
    __builtin_amdgcn_s_sleep(SLEEP);
  }
}

#define STAMP(i) do {} while (0)
#define FLUSH() do {} while (0)
#define LDS_BARRIER() do { __builtin_amdgcn_sched_barrier(0); asm volatile("s_waitcnt lgkmcnt(0)\n\ts_barrier" ::: "memory"); __builtin_amdgcn_sched_barrier(0); } while (0)

#define GATE(due_) do { __builtin_amdgcn_sched_barrier(0); \
    const unsigned d_ = (due_), e_ = tlast + GAP, due = (int)(e_ - d_) > 0 ? e_ : d_; \
    asm volatile("s_waitcnt vmcnt(0)\n" \
                 "1:\n\ts_memrealtime vcc\n\ts_waitcnt lgkmcnt(0)\n\ts_mov_b32 %0, vcc_lo\n\ts_sub_i32 vcc_lo, vcc_lo, %1\n\ts_cmp_ge_i32 vcc_lo, 0\n\ts_cbranch_scc1 2f\n\ts_sleep 1\n\ts_branch 1b\n2:" \
                 : "=&s"(tlast) : "s"(due) : "vcc", "scc", "memory"); \
    __builtin_amdgcn_sched_barrier(0); } while (0)

__device__ __forceinline__ void ln_stats(u32x4 xv, float& mean_b, float& den_b) {
  float e[8];
#pragma unroll
  for (int j = 0; j < 4; ++j) { e[2 * j] = bf_lo(xv[j]); e[2 * j + 1] = bf_hi(xv[j]); }
  float s = 0.f;
#pragma unroll
  for (int j = 0; j < 8; ++j) s += e[j];
  const float mean = wave_sum(s) * (1.0f / D);
  float v = 0.f;
#pragma unroll
  for (int j = 0; j < 8; ++j) { const float d = e[j] - mean; v = fmaf(d, d, v); }
  const float var = wave_sum(v) * (1.0f / D);
  mean_b = rbf(mean);
  den_b = rbf(sqrtf(rbf(rbf(var) + 1e-5f)));
}
__device__ __forceinline__ float ln_apply(float x, float mean_b, float den_b) { return rbf(rbf(x - mean_b) / den_b); }

__device__ __forceinline__ unsigned launch_tag() {
  const unsigned long long id = llvm_dispatch_id();
  unsigned qh = (unsigned)((unsigned long long)__builtin_amdgcn_dispatch_ptr() >> 22);
  qh *= 0x9E3779B1u; qh ^= qh >> 15;
  const unsigned t = (unsigned)id + qh;
  return ((t << 2) & 0xFFFFFE00u) | ((t << 1) & 0x000000FEu) | 1u;
}

constexpr float SCALE = 0.08838834764831845f, LOG2E = 1.4426950408889634f;

__device__ __forceinline__ float gemv64(const unsigned (&wp)[8][8], unsigned vp, int lane) {
  const unsigned x4 = dppu<0x1B>(dppu<HMIR>(vp));
  const unsigned lo = (lane & 4) ? x4 : vp, hi = (lane & 4) ? vp : x4;
  float y[8];
#pragma unroll
  for (int k = 0; k < 8; ++k) y[k] = 0.f;
#define ODOT(n, src, Q) { const unsigned ob = dppu<Q>(src); _Pragma("unroll") for (int k = 0; k < 8; ++k) y[k] = dot2(wp[n][k], ob, y[k]); }
  ODOT(0, lo, 0x00) ODOT(1, lo, 0x55) ODOT(2, lo, 0xAA) ODOT(3, lo, 0xFF)
  ODOT(4, hi, 0x00) ODOT(5, hi, 0x55) ODOT(6, hi, 0xAA) ODOT(7, hi, 0xFF)
#undef ODOT
  float a4[4], a2[2];
#pragma unroll
  for (int k = 0; k < 4; ++k) a4[k] = swap32_add(y[k], y[k + 4]);
#pragma unroll
  for (int k = 0; k < 2; ++k) a2[k] = swap16_add(a4[k], a4[k + 2]);
  const float t0 = a2[0] + dpp<ROR8>(a2[0]), t1 = a2[1] + dpp<ROR8>(a2[1]);
  return (lane & 8) ? t1 : t0;
}
__device__ __forceinline__ void pack_pair(unsigned (&w)[8], u32x4 r0, u32x4 r1) {
#pragma unroll
  for (int k = 0; k < 4; ++k) {
    w[2 * k] = __builtin_amdgcn_perm(r1[k], r0[k], 0x05040100u);
    w[2 * k + 1] = __builtin_amdgcn_perm(r1[k], r0[k], 0x07060302u);
  }
}

__global__ void __launch_bounds__(NTHR) fused_decode(const uint16_t* x, const char* kc, const char* vc,
                                                      const char* wq, const char* __restrict__ wo, const char* __restrict__ wg,
                                                      const char* __restrict__ wu, const char* __restrict__ wd, char* ws, uint16_t* __restrict__ out TUNE_ARGS) {
  __shared__ __attribute__((aligned(16))) char lds[L_END];
  const int tid = threadIdx.x, lane = tid & 63, wave = __builtin_amdgcn_readfirstlane(tid >> 6);
  const int b = blockIdx.x, xg = b & 7, j = b >> 3;
  const int h = MAPV == 4 ? (j >> 3) * 8 + ((xg + 3 * (j >> 3)) & 7) : MAPV == 3 ? (j >> 3) * 8 + xg : j;
  const int sp = MAPV >= 3 ? (j & 7) : MAPV == 0 ? xg : ((xg + j) & 7);
  STAMP(0);
  const unsigned tstart = (unsigned)__builtin_amdgcn_s_memrealtime();
  const int cb = b >> 2, kq = b & 3;
  asm volatile("" : "+s"(x), "+s"(kc), "+s"(vc), "+s"(wq));
  const bool isw = wave >= 2;
  const unsigned offa = isw ? (unsigned)(h * NSPLIT + sp) * CHUNK_BYTES : kq * (128 * H * DH * 2) + cb * 128;
  const char* pa = (isw ? kc : wq) + offa;
  const char* pb = (isw ? vc : wq + 4 * (H * DH * 2)) + offa;
  const unsigned nrec = wave == 1 ? 0 : isw ? CHUNK_BYTES : 124 * H * DH * 2;
  const unsigned s1 = isw ? 1024 : H * DH * 2;
  const unsigned voff = isw ? (wave - 2) * (TILE_KEYS * DH * 2) + lane * 16 : (lane >> 3) * (16 * H * DH * 2) + (lane & 7) * 16;
  const auto rx = __builtin_amdgcn_make_buffer_rsrc((void*)x, 0, isw ? 0 : D * 2, 0x00020000);
  u32x4 xv = __builtin_amdgcn_raw_buffer_load_b128(rx, lane * 16, 0, 0);
  const auto ra = __builtin_amdgcn_make_buffer_rsrc((void*)pa, 0, nrec, 0x00020000);
  const auto rb = __builtin_amdgcn_make_buffer_rsrc((void*)pb, 0, nrec, 0x00020000);
  u32x4 ar[RING][4], br[RING][4];
  __builtin_amdgcn_sched_barrier(0);
#pragma unroll
  for (int t = 0; t < 4; ++t) ar[0][t] = __builtin_amdgcn_raw_buffer_load_b128(ra, voff, t * s1, 2);
  __builtin_amdgcn_sched_barrier(0);
#pragma unroll
  for (int t = 0; t < 4; ++t) br[0][t] = __builtin_amdgcn_raw_buffer_load_b128(rb, voff, t * s1, 2);
  __builtin_amdgcn_sched_barrier(0);

#define ROLE_END() do { asm volatile("s_endpgm" ::: "memory"); \
    _Pragma("unroll") for (int t_ = 0; t_ < 4; ++t_) asm volatile("" : "=v"(ar[0][t_]), "=v"(br[0][t_])); \
    asm volatile("" : "=v"(xv)); } while (0)
  int role = wave;
  if (role == 0) {
    const unsigned xp = __builtin_amdgcn_raw_buffer_load_b32(rx, lane * 4, kq * 256, 0);
#pragma unroll
    for (int t = 0; t < 4; ++t) ar[1][t] = __builtin_amdgcn_raw_buffer_load_b128(ra, voff, (8 + t) * (H * DH * 2), 2);
#pragma unroll
    for (int t = 0; t < 4; ++t) br[1][t] = __builtin_amdgcn_raw_buffer_load_b128(rb, voff, (8 + t) * (H * DH * 2), 2);
    __builtin_amdgcn_sched_barrier(0);
    __builtin_amdgcn_s_setprio(3);
    gu64* G = (gu64*)ws;
    const unsigned long long warm = get(G + G_Q + b * 64 + lane);
    const int kg = lane >> 3, cc = lane & 7;
    const auto rw = __builtin_amdgcn_make_buffer_rsrc((void*)(wo + (size_t)h * (DH * D * 2) + sp * 128), 0, DH * D * 2 - sp * 128, 0x00020000);
    const unsigned wvo = kg * 16 * (D * 2) + cc * 16;
    u32x4 wv[16];
#pragma unroll
    for (int n = 0; n < 16; ++n) wv[n] = __builtin_amdgcn_raw_buffer_load_b128(rw, (MODE & 1) ? wvo : 0x40000000u, n * (D * 2), 2);
    __builtin_amdgcn_sched_barrier(0);
    STAMP(7);
    const unsigned tag = launch_tag();
    unsigned wp[8][8];
    {
      float mean_b, den_b;
      ln_stats(xv, mean_b, den_b);
      unsigned xn = pk_bf16(ln_apply(bf_lo(xp), mean_b, den_b), ln_apply(bf_hi(xp), mean_b, den_b));
      asm volatile("" : "+v"(xn));
      __builtin_amdgcn_sched_barrier(0);
      STAMP(10);
      pack_pair(wp[0], ar[0][0], ar[0][1]); pack_pair(wp[1], ar[0][2], ar[0][3]); pack_pair(wp[2], br[0][0], br[0][1]); pack_pair(wp[3], br[0][2], br[0][3]);
      pack_pair(wp[4], ar[1][0], ar[1][1]); pack_pair(wp[5], ar[1][2], ar[1][3]); pack_pair(wp[6], br[1][0], br[1][1]); pack_pair(wp[7], br[1][2], br[1][3]);
      const float qv = gemv64(wp, xn, lane);
      STAMP(11);
      putf(G + G_Q + b * 64 + lane, tag, qv);
    }
    STAMP(6);
    {
      unsigned qv[8];
      sweep<8, 1>(G + G_Q + h * 512 + lane, 64, tag, qv);
      asm volatile("" :: "v"(warm));
      const float q0 = (__builtin_bit_cast(float, qv[0]) + __builtin_bit_cast(float, qv[1])) + (__builtin_bit_cast(float, qv[2]) + __builtin_bit_cast(float, qv[3]));
      const float q1 = (__builtin_bit_cast(float, qv[4]) + __builtin_bit_cast(float, qv[5])) + (__builtin_bit_cast(float, qv[6]) + __builtin_bit_cast(float, qv[7]));
      const unsigned qq = pk_bf16(q0, q1);
      *reinterpret_cast<uint16_t*>(lds + L_Q + (cc * 8 + kg) * 2) = (uint16_t)qq;
      *reinterpret_cast<uint16_t*>(lds + L_Q + 128 + (cc * 8 + kg) * 2) = (uint16_t)(qq >> 16);
    }
    STAMP(1);
    LDS_BARRIER();
    {
      if (!(MODE & 1)) {
#pragma unroll
        for (int n = 0; n < 16; ++n) { wv[n] = __builtin_amdgcn_raw_buffer_load_b128(rw, wvo, n * (D * 2), 2); if (MODE & 4) { __builtin_amdgcn_s_sleep(64); __builtin_amdgcn_sched_barrier(0); } }
      }
#pragma unroll
      for (int n = 0; n < 8; ++n) pack_pair(wp[n], wv[2 * n], wv[2 * n + 1]);
#pragma unroll
      for (int n = 0; n < 8; ++n)
#pragma unroll
        for (int k = 0; k < 8; ++k) asm volatile("" : "+v"(wp[n][k]));
    }
    LDS_BARRIER();
    STAMP(2);
    {
      const f32x2 ml = *reinterpret_cast<const f32x2*>(lds + L_ML + (lane & 7) * 8);
      f32x2 ov[8];
#pragma unroll
      for (int w = 0; w < 8; ++w) ov[w] = *reinterpret_cast<const f32x2*>(lds + L_OP + w * 512 + lane * 8);
      const float M = oct_max(ml[0]);
      const float sw = __builtin_amdgcn_exp2f((ml[0] - M) * LOG2E);
      const float L = oct_sum(ml[1] * sw);
      float o0 = 0.f, o1 = 0.f;
#pragma unroll
      for (int w = 0; w < 8; ++w) { const float s_ = rdlane(sw, w); o0 = fmaf(s_, ov[w][0], o0); o1 = fmaf(s_, ov[w][1], o1); }
      gu64* pg = G + G_P + (h * NSPLIT + sp) * 128;
      putf(pg + lane, tag, o0);
      putf(pg + 64 + lane, tag, o1);
      if (lane < 2) putf(G + G_M + h * 16 + lane * 8 + sp, tag, lane ? L : M);
    }
    STAMP(3);
    {
      unsigned pv[18];
      gu64* pg = G + G_P + h * (NSPLIT * 128) + lane;
      gu64* mg = G + G_M + h * 16 + (lane & 7);
      for (unsigned spins = 0; spins < SPIN_CAP; ++spins) {
        bool ok = true;
#pragma unroll
        for (int k = 0; k < 18; ++k) {
          const unsigned long long v = get(k < 16 ? pg + k * 64 : mg + (k - 16) * 8);
          pv[k] = (unsigned)v; ok &= (unsigned)(v >> 32) == tag;
        }
        if (__all(ok)) break;
        __builtin_amdgcn_s_sleep(2);
      }
      STAMP(4);
      const float ms = __builtin_bit_cast(float, pv[16]), ls = __builtin_bit_cast(float, pv[17]);
      const float M = oct_max(ms);
      const float sw = __builtin_amdgcn_exp2f((ms - M) * LOG2E);
      const float L = oct_sum(ls * sw);
      const float wgt = sw / L;
      float o0 = 0.f, o1 = 0.f;
#pragma unroll
      for (int s = 0; s < 8; ++s) { const float s_ = rdlane(wgt, s); o0 = fmaf(s_, __builtin_bit_cast(float, pv[2 * s]), o0); o1 = fmaf(s_, __builtin_bit_cast(float, pv[2 * s + 1]), o1); }
      const float yv = gemv64(wp, pk_bf16(o0, o1), lane);
      putf(G + G_Y + (sp * YROWS + h) * 64 + lane, tag, yv);
    }
    STAMP(5);
    FLUSH();
    ROLE_END();
  }

  asm volatile("" : "+s"(role));
  if (role == 1) {
    const unsigned tag = launch_tag();
    gu64* G = (gu64*)ws;
    const int u = xg * FFN_PER_X + j;
    const bool ffn = !NO_FFN && (j < FFN_PER_X) && (u < FFN_UNITS);
    const int kf = u / 43, cu = u - kf * 43;
    u32x4 gw[8], uw[8];
    unsigned xp;
    {
      const unsigned o = kf * (128 * F * 2) + cu * 64, n = ffn ? 128 * F * 2 - cu * 64 : 0;
      const auto rg_ = __builtin_amdgcn_make_buffer_rsrc((void*)(wg + o), 0, n, 0x00020000);
      const auto ru_ = __builtin_amdgcn_make_buffer_rsrc((void*)(wu + o), 0, n, 0x00020000);
      const unsigned vo = (lane >> 2) * (8 * F * 2) + (lane & 3) * 16;
      xp = __builtin_amdgcn_raw_buffer_load_b32(rx, lane * 4, (kf & 3) * 256, 0);
#pragma unroll
      for (int i = 0; i < 8; ++i) {
        gw[i] = __builtin_amdgcn_raw_buffer_load_b128(rg_, vo, i * (F * 2), 2);
        uw[i] = __builtin_amdgcn_raw_buffer_load_b128(ru_, vo, i * (F * 2), 2);
      }
    }
    LDS_BARRIER();
    if (ffn) {
      float mean_b, den_b;
      ln_stats(xv, mean_b, den_b);
      const unsigned xn = pk_bf16(ln_apply(bf_lo(xp), mean_b, den_b), ln_apply(bf_hi(xp), mean_b, den_b));
      float g[8], up[8];
#pragma unroll
      for (int k = 0; k < 8; ++k) { g[k] = 0.f; up[k] = 0.f; }
#define FDOT(n, Q) { const unsigned ob = dppu<Q>(xn); unsigned w[8]; \
      pack_pair(w, gw[2 * (n)], gw[2 * (n) + 1]); _Pragma("unroll") for (int k = 0; k < 8; ++k) g[k] = dot2(w[k], ob, g[k]); \
      pack_pair(w, uw[2 * (n)], uw[2 * (n) + 1]); _Pragma("unroll") for (int k = 0; k < 8; ++k) up[k] = dot2(w[k], ob, up[k]); }
      FDOT(0, 0x00) FDOT(1, 0x55) FDOT(2, 0xAA) FDOT(3, 0xFF)
      float a8[8], a4[4];
#pragma unroll
      for (int k = 0; k < 8; ++k) a8[k] = swap32_add(g[k], up[k]);
#pragma unroll
      for (int k = 0; k < 4; ++k) { float t = swap16_add(a8[k], a8[k + 4]); t += dpp<ROR8>(t); t += dpp<ROR4>(t); a4[k] = t; }
      const int sel = (lane >> 2) & 3;
      const float v = sel == 0 ? a4[0] : sel == 1 ? a4[1] : sel == 2 ? a4[2] : a4[3];
      putf(G + G_A + (cu * 4 + kf) * 64 + lane, tag, v);
    }
    if (!NO_FFN && j >= 21) {
      const int fg = j - 21, kg = lane >> 3, cc = lane & 7;
      unsigned wp[8][8];
      {
        const auto rw = __builtin_amdgcn_make_buffer_rsrc((void*)(wd + (size_t)fg * (128 * D * 2) + xg * 128), 0, (F - fg * 128) * (D * 2) - xg * 128, 0x00020000);
        const unsigned o = (32 * (kg >> 2) + 4 * ((kg >> 1) & 1) + 2 * (kg & 1)) * (D * 2) + cc * 16;
        u32x4 wv[16];
#pragma unroll
        for (int n = 0; n < 8; ++n) {
          wv[2 * n] = __builtin_amdgcn_raw_buffer_load_b128(rw, o + (8 * (n & 3) + (n >> 2)) * (D * 2), 0, 2);
          wv[2 * n + 1] = __builtin_amdgcn_raw_buffer_load_b128(rw, o + (64 + 8 * (n & 3) + (n >> 2)) * (D * 2), 0, 2);
        }
#pragma unroll
        for (int n = 0; n < 8; ++n) pack_pair(wp[n], wv[2 * n], wv[2 * n + 1]);
      }
      unsigned av[12], aw[4];
      sweep<12, 16>(G + G_A + fg * 1024 + lane, 64, tag, av);
      sweep<4, 16>(G + G_A + fg * 1024 + (fg == 10 ? 512 : 768) + lane, 64, tag, aw);
      float v[4];
#pragma unroll
      for (int i = 0; i < 3; ++i) v[i] = (__builtin_bit_cast(float, av[4 * i]) + __builtin_bit_cast(float, av[4 * i + 1])) + (__builtin_bit_cast(float, av[4 * i + 2]) + __builtin_bit_cast(float, av[4 * i + 3]));
      v[3] = (__builtin_bit_cast(float, aw[0]) + __builtin_bit_cast(float, aw[1])) + (__builtin_bit_cast(float, aw[2]) + __builtin_bit_cast(float, aw[3]));
      float hv[2];
#pragma unroll
      for (int i = 0; i < 2; ++i) {
        auto sw = __builtin_amdgcn_permlane32_swap(__float_as_uint(v[2 * i]), __float_as_uint(v[2 * i + 1]), false, false);
        const float gg = rbf(__uint_as_float(sw[0])), uu = rbf(__uint_as_float(sw[1]));
        const float inner = 0.7978845608028654f * (gg + 0.044715f * gg * gg * gg);
        hv[i] = rbf(0.5f * gg * (1.0f + tanhf(inner))) * uu;
      }
      const float yv = gemv64(wp, pk_bf16(hv[0], hv[1]), lane);
      putf(G + G_Y + (xg * YROWS + H + fg) * 64 + lane, tag, yv);
    }
    STAMP(1);
    if (j < 28) {
      LDS_BARRIER();
      FLUSH();
    } else {
      const int qd = j - 28, hg = lane >> 4, li = qd * 16 + (lane & 15), col = xg * 64 + (li & 7) * 8 + (li >> 3);
      gu64* yg = G + G_Y + (xg * YROWS) * 64 + li;
      const float xo = bf_lo(__builtin_amdgcn_raw_buffer_load_b16(rx, col * 2, 0, 0));
      float fs = 0.f;
      if (!NO_FFN) {
        unsigned fv[2], fw[1];
        sweep<2, 16>(yg + (H + hg) * 64, 256, tag, fv);
        sweep<1, 16>(yg + (H + 8 + (hg == 3 ? 0 : hg)) * 64, 0, tag, fw);
        fs = (__builtin_bit_cast(float, fv[0]) + __builtin_bit_cast(float, fv[1])) + (hg == 3 ? 0.f : __builtin_bit_cast(float, fw[0]));
      }
      fs = rbf(xrow_sum(fs));
      LDS_BARRIER();
      unsigned yv[8];
      sweep<8, 1>(yg + hg * 64, 256, tag, yv);
      STAMP(4);
      float a = 0.f;
#pragma unroll
      for (int i = 0; i < 8; ++i) a += __builtin_bit_cast(float, yv[i]);
      a = rbf(xrow_sum(a));
      const float o = rbf(xo + a) + fs;
      if (lane < 16) out[col] = (uint16_t)pk_bf16(o, o);
      STAMP(5);
      FLUSH();
    }
    ROLE_END();
  }

  {
    const int wi = wave - 2, r = lane >> 4, c = lane & 15;
#define kr ar
#define vr br
#define ISSUE_K(slot, st) _Pragma("unroll") for (int t_ = 0; t_ < 4; ++t_) kr[slot][t_] = __builtin_amdgcn_raw_buffer_load_b128(ra, voff + t_ * 1024, (st) * STEP_BYTES, 2)
#define ISSUE_V(slot, st) _Pragma("unroll") for (int t_ = 0; t_ < 4; ++t_) vr[slot][t_] = __builtin_amdgcn_raw_buffer_load_b128(rb, voff + t_ * 1024, (st) * STEP_BYTES, 2)
    const int tw = (wi - LEAD) * SLOT;
    unsigned tlast = tstart;
#define DUE(n) (tstart + (unsigned)((tw + (n) * (WORKERS * SLOT)) >> 8))
#pragma unroll
    for (int g = 1; g < RING; ++g) {
      GATE(DUE(2 * g)); ISSUE_K(g, g);
      GATE(DUE(2 * g + 1)); ISSUE_V(g, g);
    }
    STAMP(7);
    LDS_BARRIER();
    const u32x4 qp = *reinterpret_cast<const u32x4*>(lds + L_Q + c * 16);
    STAMP(1);
    float m = -INFINITY, l = 0.f, acc[8];
#pragma unroll
    for (int k = 0; k < 8; ++k) acc[k] = 0.f;

#define PIN_ACC() asm volatile("" : "+v"(acc[0]), "+v"(acc[1]), "+v"(acc[2]), "+v"(acc[3]), "+v"(acc[4]), "+v"(acc[5]), "+v"(acc[6]), "+v"(acc[7]), "+v"(l), "+v"(m))
#define PIN_S() asm volatile("" : "+v"(s[0]), "+v"(s[1]), "+v"(s[2]), "+v"(s[3]))
#define TILE(slot, st, ISSUE) do { \
    float s[4]; \
    _Pragma("unroll") for (int t = 0; t < 4; ++t) { \
      float d = dot2(kr[slot][t][0], qp[0], 0.f); d = dot2(kr[slot][t][1], qp[1], d); d = dot2(kr[slot][t][2], qp[2], d); s[t] = dot2(kr[slot][t][3], qp[3], d); } \
    __builtin_amdgcn_sched_barrier(0); \
    if (ISSUE) { PIN_S(); GATE(DUE(2 * (st))); ISSUE_K(slot, st); } \
    __builtin_amdgcn_sched_barrier(0); \
    _Pragma("unroll") for (int t = 0; t < 4; ++t) s[t] = row16_sum(s[t]); \
    { const unsigned p01 = pk_bf16(s[0], s[1]), p23 = pk_bf16(s[2], s[3]); \
      const unsigned r01 = pk_bf16(bf_lo(p01) * SCALE, bf_hi(p01) * SCALE), r23 = pk_bf16(bf_lo(p23) * SCALE, bf_hi(p23) * SCALE); \
      s[0] = bf_lo(r01); s[1] = bf_hi(r01); s[2] = bf_lo(r23); s[3] = bf_hi(r23); } \
    const float mn = fmaxf(fmaxf(fmaxf(s[0], s[1]), fmaxf(s[2], s[3])), m); \
    const float nb = -mn * LOG2E; \
    const float al = __builtin_amdgcn_exp2f(fmaf(m, LOG2E, nb)); \
    m = mn; \
    float p[4]; \
    _Pragma("unroll") for (int t = 0; t < 4; ++t) p[t] = __builtin_amdgcn_exp2f(fmaf(s[t], LOG2E, nb)); \
    l = fmaf(l, al, (p[0] + p[1]) + (p[2] + p[3])); \
    _Pragma("unroll") for (int k = 0; k < 8; ++k) acc[k] *= al; \
    _Pragma("unroll") for (int t = 0; t < 4; ++t) \
      _Pragma("unroll") for (int k = 0; k < 4; ++k) { acc[2 * k] = fmaf(p[t], bf_lo(vr[slot][t][k]), acc[2 * k]); acc[2 * k + 1] = fmaf(p[t], bf_hi(vr[slot][t][k]), acc[2 * k + 1]); } \
    __builtin_amdgcn_sched_barrier(0); \
    if (ISSUE) { PIN_ACC(); GATE(DUE(2 * (st) + 1)); ISSUE_V(slot, st); } \
    __builtin_amdgcn_sched_barrier(0); \
  } while (0)

    static_assert(RING == 3 && STEPS % 3 == 2, "loop and tail below");
#pragma unroll 1
    for (int st = RING; st + 2 < STEPS; st += 3) {
      TILE(0, st, true);
      TILE(1, st + 1, true);
      TILE(2, st + 2, true);
    }
    TILE(0, STEPS - 2, true);
    TILE(1, STEPS - 1, true);
    TILE(2, 0, false);
    TILE(0, 0, false);
    TILE(1, 0, false);
    STAMP(2);
    const float mw = xrow_max(m);
    const float sc = __builtin_amdgcn_exp2f((m - mw) * LOG2E);
    l = xrow_sum(l * sc);
    float a4[4], a2[2];
#pragma unroll
    for (int k = 0; k < 4; ++k) a4[k] = swap32_add(acc[k] * sc, acc[k + 4] * sc);
#pragma unroll
    for (int k = 0; k < 2; ++k) a2[k] = swap16_add(a4[k], a4[k + 2]);
    *reinterpret_cast<f32x2*>(lds + L_OP + wi * 512 + c * 32 + r * 8) = f32x2{a2[0], a2[1]};
    if (lane == 0) *reinterpret_cast<f32x2*>(lds + L_ML + wi * 8) = f32x2{mw, l};
    LDS_BARRIER();
    FLUSH();
#undef kr
#undef vr
  }

}

}

extern "C" void kernel_launch(void* const* d_in, const int* in_sizes, int n_in, void* d_out, int out_size, void* d_ws, size_t ws_size,
                              hipStream_t stream) {
  fused_decode<<<NWG, NTHR, 0, stream>>>((const uint16_t*)d_in[0], (const char*)d_in[1], (const char*)d_in[2], (const char*)d_in[3],
                                         (const char*)d_in[4], (const char*)d_in[6], (const char*)d_in[7], (const char*)d_in[8],
                                         (char*)d_ws, (uint16_t*)d_out TUNE_PASS);
}
```

```cpp
#define ORIGIN 0
#include <hip/hip_runtime.h>
#include <hip/hip_bf16.h>
#include <stdint.h>

#define PRIO_MODE 0
#define NO_FFN 0
#define MAPV 4
#define NO_COMP 0
#define TUNE_ARGS
#define TUNE_PASS
constexpr int SLOT = 4000, LEAD = -8, GAP = 0, MODE = 1;
namespace {

constexpr int D = 512, H = 32, SK = 32768, DH = 128, F = 1376;
constexpr int NWG = 256, NSPLIT = 8, WORKERS = 8, NWAVES = WORKERS + 2, NTHR = NWAVES * 64;
constexpr int KEYS_WG = SK / NSPLIT;
constexpr int TILE_KEYS = 16;
constexpr int STEPS = KEYS_WG / (WORKERS * TILE_KEYS);
constexpr int STEP_BYTES = WORKERS * TILE_KEYS * DH * 2;
constexpr int CHUNK_BYTES = KEYS_WG * DH * 2;
constexpr int FFN_UNITS = F / 8;
constexpr int FFN_PER_X = 22;
constexpr int RING = 3;
constexpr unsigned SPIN_CAP = 1u << 20;

constexpr int G_Q = 0;
constexpr int G_M = G_Q + H * 512;
constexpr int G_P = G_M + H * 16;
constexpr int YROWS = 44;
constexpr int G_Y = G_P + NWG * 128;
constexpr int G_A = G_Y + 8 * YROWS * 64;
constexpr int G_T = G_A + 43 * 256;

constexpr int L_Q = 0;
constexpr int L_T0 = 4416;
constexpr int L_ML = 256;
constexpr int L_OP = 320;
constexpr int L_END = L_OP + 4096 + 16;

typedef unsigned u32x4 __attribute__((ext_vector_type(4)));
typedef float f32x2 __attribute__((ext_vector_type(2)));
typedef float f32x4 __attribute__((ext_vector_type(4)));
typedef __bf16 bf16x2 __attribute__((ext_vector_type(2)));
typedef __attribute__((address_space(1))) unsigned long long gu64;

extern "C" __device__ unsigned long long llvm_dispatch_id() __asm("llvm.amdgcn.dispatch.id");

__device__ __forceinline__ float dot2(unsigned a, unsigned b, float acc) {
  return __builtin_amdgcn_fdot2_f32_bf16(__builtin_bit_cast(bf16x2, a), __builtin_bit_cast(bf16x2, b), acc, false);
}
__device__ __forceinline__ unsigned pk_bf16(float lo, float hi) {
  f32x2 v = {lo, hi};
  return __builtin_bit_cast(unsigned, __builtin_convertvector(v, bf16x2));
}
__device__ __forceinline__ float bf_lo(unsigned x) { return __builtin_bit_cast(float, x << 16); }
__device__ __forceinline__ float bf_hi(unsigned x) { return __builtin_bit_cast(float, x & 0xffff0000u); }
__device__ __forceinline__ float rbf(float x) { return bf_lo(pk_bf16(x, x)); }

template <int CTRL> __device__ __forceinline__ unsigned dppu(unsigned x) { return (unsigned)__builtin_amdgcn_mov_dpp((int)x, CTRL, 0xf, 0xf, true); }
template <int CTRL> __device__ __forceinline__ float dpp(float x) { return __builtin_bit_cast(float, dppu<CTRL>(__builtin_bit_cast(unsigned, x))); }
constexpr int XOR1 = 0xB1, XOR2 = 0x4E, HMIR = 0x141, ROR4 = 0x124, ROR8 = 0x128;
__device__ __forceinline__ float row16_sum(float x) {
  x += dpp<XOR1>(x); x += dpp<XOR2>(x); x += dpp<HMIR>(x); x += dpp<ROR8>(x); return x;
}
__device__ __forceinline__ float oct_sum(float x) { x += dpp<XOR1>(x); x += dpp<XOR2>(x); x += dpp<HMIR>(x); return x; }
__device__ __forceinline__ float oct_max(float x) { x = fmaxf(x, dpp<XOR1>(x)); x = fmaxf(x, dpp<XOR2>(x)); x = fmaxf(x, dpp<HMIR>(x)); return x; }
__device__ __forceinline__ float swap16_add(float a, float b) {
  auto s = __builtin_amdgcn_permlane16_swap(__float_as_uint(a), __float_as_uint(b), false, false);
  return __uint_as_float(s[0]) + __uint_as_float(s[1]);
}
__device__ __forceinline__ float swap32_add(float a, float b) {
  auto s = __builtin_amdgcn_permlane32_swap(__float_as_uint(a), __float_as_uint(b), false, false);
  return __uint_as_float(s[0]) + __uint_as_float(s[1]);
}
__device__ __forceinline__ float xrow_sum(float x) { x = swap16_add(x, x); return swap32_add(x, x); }
__device__ __forceinline__ float xrow_max(float x) {
  auto s = __builtin_amdgcn_permlane16_swap(__float_as_uint(x), __float_as_uint(x), false, false);
  x = fmaxf(__uint_as_float(s[0]), __uint_as_float(s[1]));
  auto t = __builtin_amdgcn_permlane32_swap(__float_as_uint(x), __float_as_uint(x), false, false);
  return fmaxf(__uint_as_float(t[0]), __uint_as_float(t[1]));
}
__device__ __forceinline__ float wave_sum(float x) { return xrow_sum(row16_sum(x)); }
__device__ __forceinline__ float rdlane(float x, int l) { return __builtin_bit_cast(float, __builtin_amdgcn_readlane(__builtin_bit_cast(int, x), l)); }

__device__ __forceinline__ void put(gu64* g, unsigned tag, unsigned v) {
  __hip_atomic_store(g, ((unsigned long long)tag << 32) | v, __ATOMIC_RELAXED, __HIP_MEMORY_SCOPE_AGENT);
}
__device__ __forceinline__ void putf(gu64* g, unsigned tag, float v) { put(g, tag, __builtin_bit_cast(unsigned, v)); }
__device__ __forceinline__ unsigned long long get(gu64* g) { return __hip_atomic_load(g, __ATOMIC_RELAXED, __HIP_MEMORY_SCOPE_AGENT); }
template <int N, int SLEEP> __device__ __forceinline__ void sweep(gu64* g, int S, unsigned tag, unsigned (&v)[N]) {
  for (unsigned spins = 0; spins < SPIN_CAP; ++spins) {
    bool ok = true;
#pragma unroll
    for (int k = 0; k < N; ++k) { const unsigned long long x = get(g + k * S); v[k] = (unsigned)x; ok &= (unsigned)(x >> 32) == tag; }
    if (__all(ok)) return;
    __builtin_amdgcn_s_sleep(SLEEP);
  }
}

#define STAMP(i) do {} while (0)
#define FLUSH() do {} while (0)
#define LDS_BARRIER() do { __builtin_amdgcn_sched_barrier(0); asm volatile("s_waitcnt lgkmcnt(0)\n\ts_barrier" ::: "memory"); __builtin_amdgcn_sched_barrier(0); } while (0)

#define GATE(due_) do { __builtin_amdgcn_sched_barrier(0); \
    const unsigned d_ = (due_), e_ = tlast + GAP, due = (int)(e_ - d_) > 0 ? e_ : d_; \
    asm volatile("s_waitcnt vmcnt(0)\n" \
                 "1:\n\ts_memrealtime vcc\n\ts_waitcnt lgkmcnt(0)\n\ts_mov_b32 %0, vcc_lo\n\ts_sub_i32 vcc_lo, vcc_lo, %1\n\ts_cmp_ge_i32 vcc_lo, 0\n\ts_cbranch_scc1 2f\n\ts_sleep 1\n\ts_branch 1b\n2:" \
                 : "=&s"(tlast) : "s"(due) : "vcc", "scc", "memory"); \
    __builtin_amdgcn_sched_barrier(0); } while (0)

__device__ __forceinline__ void ln_stats(u32x4 xv, float& mean_b, float& den_b) {
  float e[8];
#pragma unroll
  for (int j = 0; j < 4; ++j) { e[2 * j] = bf_lo(xv[j]); e[2 * j + 1] = bf_hi(xv[j]); }
  float s = 0.f;
#pragma unroll
  for (int j = 0; j < 8; ++j) s += e[j];
  const float mean = wave_sum(s) * (1.0f / D);
  float v = 0.f;
#pragma unroll
  for (int j = 0; j < 8; ++j) { const float d = e[j] - mean; v = fmaf(d, d, v); }
  const float var = wave_sum(v) * (1.0f / D);
  mean_b = rbf(mean);
  den_b = rbf(sqrtf(rbf(rbf(var) + 1e-5f)));
}
__device__ __forceinline__ float ln_apply(float x, float mean_b, float den_b) { return rbf(rbf(x - mean_b) / den_b); }

__device__ __forceinline__ unsigned launch_tag() {
  const unsigned long long id = llvm_dispatch_id();
  unsigned qh = (unsigned)((unsigned long long)__builtin_amdgcn_dispatch_ptr() >> 22);
  qh *= 0x9E3779B1u; qh ^= qh >> 15;
  const unsigned t = (unsigned)id + qh;
  return ((t << 2) & 0xFFFFFE00u) | ((t << 1) & 0x000000FEu) | 1u;
}

constexpr float SCALE = 0.08838834764831845f, LOG2E = 1.4426950408889634f;

__device__ __forceinline__ float gemv64(const unsigned (&wp)[8][8], unsigned vp, int lane) {
  const unsigned x4 = dppu<0x1B>(dppu<HMIR>(vp));
  const unsigned lo = (lane & 4) ? x4 : vp, hi = (lane & 4) ? vp : x4;
  float y[8];
#pragma unroll
  for (int k = 0; k < 8; ++k) y[k] = 0.f;
#define ODOT(n, src, Q) { const unsigned ob = dppu<Q>(src); _Pragma("unroll") for (int k = 0; k < 8; ++k) y[k] = dot2(wp[n][k], ob, y[k]); }
  ODOT(0, lo, 0x00) ODOT(1, lo, 0x55) ODOT(2, lo, 0xAA) ODOT(3, lo, 0xFF)
  ODOT(4, hi, 0x00) ODOT(5, hi, 0x55) ODOT(6, hi, 0xAA) ODOT(7, hi, 0xFF)
#undef ODOT
  float a4[4], a2[2];
#pragma unroll
  for (int k = 0; k < 4; ++k) a4[k] = swap32_add(y[k], y[k + 4]);
#pragma unroll
  for (int k = 0; k < 2; ++k) a2[k] = swap16_add(a4[k], a4[k + 2]);
  const float t0 = a2[0] + dpp<ROR8>(a2[0]), t1 = a2[1] + dpp<ROR8>(a2[1]);
  return (lane & 8) ? t1 : t0;
}
__device__ __forceinline__ void pack_pair(unsigned (&w)[8], u32x4 r0, u32x4 r1) {
#pragma unroll
  for (int k = 0; k < 4; ++k) {
    w[2 * k] = __builtin_amdgcn_perm(r1[k], r0[k], 0x05040100u);
    w[2 * k + 1] = __builtin_amdgcn_perm(r1[k], r0[k], 0x07060302u);
  }
}

__global__ void __launch_bounds__(NTHR) fused_decode(const uint16_t* x, const char* kc, const char* vc,
                                                      const char* wq, const char* __restrict__ wo, const char* __restrict__ wg,
                                                      const char* __restrict__ wu, const char* __restrict__ wd, char* ws, uint16_t* __restrict__ out TUNE_ARGS) {
  __shared__ __attribute__((aligned(16))) char lds[L_END];
  const int tid = threadIdx.x, lane = tid & 63, wave = __builtin_amdgcn_readfirstlane(tid >> 6);
  const int b = blockIdx.x, xg = b & 7, j = b >> 3;
  const int h = MAPV == 4 ? (j >> 3) * 8 + ((xg + 3 * (j >> 3)) & 7) : MAPV == 3 ? (j >> 3) * 8 + xg : j;
  const int sp = MAPV >= 3 ? (j & 7) : MAPV == 0 ? xg : ((xg + j) & 7);
  STAMP(0);
  const unsigned tstart = (unsigned)__builtin_amdgcn_s_memrealtime();
  const int cb = b >> 2, kq = b & 3;
  asm volatile("" : "+s"(x), "+s"(kc), "+s"(vc), "+s"(wq));
  const bool isw = wave >= 2;
  const unsigned offa = isw ? (unsigned)(h * NSPLIT + sp) * CHUNK_BYTES : kq * (128 * H * DH * 2) + cb * 128;
  const char* pa = (isw ? kc : wq) + offa;
  const char* pb = (isw ? vc : wq + 4 * (H * DH * 2)) + offa;
  const unsigned nrec = wave == 1 ? 0 : isw ? CHUNK_BYTES : 124 * H * DH * 2;
  const unsigned s1 = isw ? 1024 : H * DH * 2;
  const unsigned voff = isw ? (wave - 2) * (TILE_KEYS * DH * 2) + lane * 16 : (lane >> 3) * (16 * H * DH * 2) + (lane & 7) * 16;
  const auto rx = __builtin_amdgcn_make_buffer_rsrc((void*)x, 0, isw ? 0 : D * 2, 0x00020000);
  u32x4 xv = __builtin_amdgcn_raw_buffer_load_b128(rx, lane * 16, 0, 0);
  const auto ra = __builtin_amdgcn_make_buffer_rsrc((void*)pa, 0, nrec, 0x00020000);
  const auto rb = __builtin_amdgcn_make_buffer_rsrc((void*)pb, 0, nrec, 0x00020000);
  u32x4 ar[RING][4], br[RING][4];
  __builtin_amdgcn_sched_barrier(0);
#pragma unroll
  for (int t = 0; t < 4; ++t) ar[0][t] = __builtin_amdgcn_raw_buffer_load_b128(ra, voff, t * s1, 2);
  __builtin_amdgcn_sched_barrier(0);
#pragma unroll
  for (int t = 0; t < 4; ++t) br[0][t] = __builtin_amdgcn_raw_buffer_load_b128(rb, voff, t * s1, 2);
  __builtin_amdgcn_sched_barrier(0);

#define ROLE_END() do { asm volatile("s_endpgm" ::: "memory"); \
    _Pragma("unroll") for (int t_ = 0; t_ < 4; ++t_) asm volatile("" : "=v"(ar[0][t_]), "=v"(br[0][t_])); \
    asm volatile("" : "=v"(xv)); } while (0)
  int role = wave;
  if (role == 0) {
    const unsigned xp = __builtin_amdgcn_raw_buffer_load_b32(rx, lane * 4, kq * 256, 0);
#pragma unroll
    for (int t = 0; t < 4; ++t) ar[1][t] = __builtin_amdgcn_raw_buffer_load_b128(ra, voff, (8 + t) * (H * DH * 2), 2);
#pragma unroll
    for (int t = 0; t < 4; ++t) br[1][t] = __builtin_amdgcn_raw_buffer_load_b128(rb, voff, (8 + t) * (H * DH * 2), 2);
    __builtin_amdgcn_sched_barrier(0);
    __builtin_amdgcn_s_setprio(3);
    gu64* G = (gu64*)ws;
    const unsigned long long warm = get(G + G_Q + b * 64 + lane);
    const int kg = lane >> 3, cc = lane & 7;
    const auto rw = __builtin_amdgcn_make_buffer_rsrc((void*)(wo + (size_t)h * (DH * D * 2) + sp * 128), 0, DH * D * 2 - sp * 128, 0x00020000);
    const unsigned wvo = kg * 16 * (D * 2) + cc * 16;
    u32x4 wv[16];
#pragma unroll
    for (int n = 0; n < 16; ++n) wv[n] = __builtin_amdgcn_raw_buffer_load_b128(rw, (MODE & 1) ? wvo : 0x40000000u, n * (D * 2), 2);
    __builtin_amdgcn_sched_barrier(0);
    STAMP(7);
    const unsigned tag = launch_tag();
    unsigned wp[8][8];
    {
      float mean_b, den_b;
      ln_stats(xv, mean_b, den_b);
      unsigned xn = pk_bf16(ln_apply(bf_lo(xp), mean_b, den_b), ln_apply(bf_hi(xp), mean_b, den_b));
      asm volatile("" : "+v"(xn));
      __builtin_amdgcn_sched_barrier(0);
      STAMP(10);
      pack_pair(wp[0], ar[0][0], ar[0][1]); pack_pair(wp[1], ar[0][2], ar[0][3]); pack_pair(wp[2], br[0][0], br[0][1]); pack_pair(wp[3], br[0][2], br[0][3]);
      pack_pair(wp[4], ar[1][0], ar[1][1]); pack_pair(wp[5], ar[1][2], ar[1][3]); pack_pair(wp[6], br[1][0], br[1][1]); pack_pair(wp[7], br[1][2], br[1][3]);
      const float qv = gemv64(wp, xn, lane);
      STAMP(11);
      putf(G + G_Q + b * 64 + lane, tag, qv);
      if (lane == 0) put(G + G_T + b, tag, tstart);
    }
    STAMP(6);
    {
      unsigned qv[8];
      sweep<8, 1>(G + G_Q + h * 512 + lane, 64, tag, qv);
      asm volatile("" :: "v"(warm));
      unsigned tv[1];
      sweep<1, 1>(G + G_T + h * 8 + (lane & 7), 0, tag, tv);
      int dt = (int)(tv[0] - tstart);
      if (ORIGIN == 1) { dt = min(dt, (int)dppu<XOR1>(dt)); dt = min(dt, (int)dppu<XOR2>(dt)); dt = min(dt, (int)dppu<HMIR>(dt)); }
      else if (ORIGIN == 2) { dt += (int)dppu<XOR1>(dt); dt += (int)dppu<XOR2>(dt); dt += (int)dppu<HMIR>(dt); dt >>= 3; }
      else dt = 0;
      if (lane == 0) *reinterpret_cast<unsigned*>(lds + L_T0) = tstart + dt;
      const float q0 = (__builtin_bit_cast(float, qv[0]) + __builtin_bit_cast(float, qv[1])) + (__builtin_bit_cast(float, qv[2]) + __builtin_bit_cast(float, qv[3]));
      const float q1 = (__builtin_bit_cast(float, qv[4]) + __builtin_bit_cast(float, qv[5])) + (__builtin_bit_cast(float, qv[6]) + __builtin_bit_cast(float, qv[7]));
      const unsigned qq = pk_bf16(q0, q1);
      *reinterpret_cast<uint16_t*>(lds + L_Q + (cc * 8 + kg) * 2) = (uint16_t)qq;
      *reinterpret_cast<uint16_t*>(lds + L_Q + 128 + (cc * 8 + kg) * 2) = (uint16_t)(qq >> 16);
    }
    STAMP(1);
    LDS_BARRIER();
    {
      if (!(MODE & 1)) {
#pragma unroll
        for (int n = 0; n < 16; ++n) { wv[n] = __builtin_amdgcn_raw_buffer_load_b128(rw, wvo, n * (D * 2), 2); if (MODE & 4) { __builtin_amdgcn_s_sleep(64); __builtin_amdgcn_sched_barrier(0); } }
      }
#pragma unroll
      for (int n = 0; n < 8; ++n) pack_pair(wp[n], wv[2 * n], wv[2 * n + 1]);
#pragma unroll
      for (int n = 0; n < 8; ++n)
#pragma unroll
        for (int k = 0; k < 8; ++k) asm volatile("" : "+v"(wp[n][k]));
    }
    LDS_BARRIER();
    STAMP(2);
    {
      const f32x2 ml = *reinterpret_cast<const f32x2*>(lds + L_ML + (lane & 7) * 8);
      f32x2 ov[8];
#pragma unroll
      for (int w = 0; w < 8; ++w) ov[w] = *reinterpret_cast<const f32x2*>(lds + L_OP + w * 512 + lane * 8);
      const float M = oct_max(ml[0]);
      const float sw = __builtin_amdgcn_exp2f((ml[0] - M) * LOG2E);
      const float L = oct_sum(ml[1] * sw);
      float o0 = 0.f, o1 = 0.f;
#pragma unroll
      for (int w = 0; w < 8; ++w) { const float s_ = rdlane(sw, w); o0 = fmaf(s_, ov[w][0], o0); o1 = fmaf(s_, ov[w][1], o1); }
      gu64* pg = G + G_P + (h * NSPLIT + sp) * 128;
      putf(pg + lane, tag, o0);
      putf(pg + 64 + lane, tag, o1);
      if (lane < 2) putf(G + G_M + h * 16 + lane * 8 + sp, tag, lane ? L : M);
    }
    STAMP(3);
    {
      unsigned pv[18];
      gu64* pg = G + G_P + h * (NSPLIT * 128) + lane;
      gu64* mg = G + G_M + h * 16 + (lane & 7);
      for (unsigned spins = 0; spins < SPIN_CAP; ++spins) {
        bool ok = true;
#pragma unroll
        for (int k = 0; k < 18; ++k) {
          const unsigned long long v = get(k < 16 ? pg + k * 64 : mg + (k - 16) * 8);
          pv[k] = (unsigned)v; ok &= (unsigned)(v >> 32) == tag;
        }
        if (__all(ok)) break;
        __builtin_amdgcn_s_sleep(2);
      }
      STAMP(4);
      const float ms = __builtin_bit_cast(float, pv[16]), ls = __builtin_bit_cast(float, pv[17]);
      const float M = oct_max(ms);
      const float sw = __builtin_amdgcn_exp2f((ms - M) * LOG2E);
      const float L = oct_sum(ls * sw);
      const float wgt = sw / L;
      float o0 = 0.f, o1 = 0.f;
#pragma unroll
      for (int s = 0; s < 8; ++s) { const float s_ = rdlane(wgt, s); o0 = fmaf(s_, __builtin_bit_cast(float, pv[2 * s]), o0); o1 = fmaf(s_, __builtin_bit_cast(float, pv[2 * s + 1]), o1); }
      const float yv = gemv64(wp, pk_bf16(o0, o1), lane);
      putf(G + G_Y + (sp * YROWS + h) * 64 + lane, tag, yv);
    }
    STAMP(5);
    FLUSH();
    ROLE_END();
  }

  asm volatile("" : "+s"(role));
  if (role == 1) {
    const unsigned tag = launch_tag();
    gu64* G = (gu64*)ws;
    const int u = xg * FFN_PER_X + j;
    const bool ffn = !NO_FFN && (j < FFN_PER_X) && (u < FFN_UNITS);
    const int kf = u / 43, cu = u - kf * 43;
    u32x4 gw[8], uw[8];
    unsigned xp;
    {
      const unsigned o = kf * (128 * F * 2) + cu * 64, n = ffn ? 128 * F * 2 - cu * 64 : 0;
      const auto rg_ = __builtin_amdgcn_make_buffer_rsrc((void*)(wg + o), 0, n, 0x00020000);
      const auto ru_ = __builtin_amdgcn_make_buffer_rsrc((void*)(wu + o), 0, n, 0x00020000);
      const unsigned vo = (lane >> 2) * (8 * F * 2) + (lane & 3) * 16;
      xp = __builtin_amdgcn_raw_buffer_load_b32(rx, lane * 4, (kf & 3) * 256, 0);
#pragma unroll
      for (int i = 0; i < 8; ++i) {
        gw[i] = __builtin_amdgcn_raw_buffer_load_b128(rg_, vo, i * (F * 2), 2);
        uw[i] = __builtin_amdgcn_raw_buffer_load_b128(ru_, vo, i * (F * 2), 2);
      }
    }
    LDS_BARRIER();
    if (ffn) {
      float mean_b, den_b;
      ln_stats(xv, mean_b, den_b);
      const unsigned xn = pk_bf16(ln_apply(bf_lo(xp), mean_b, den_b), ln_apply(bf_hi(xp), mean_b, den_b));
      float g[8], up[8];
#pragma unroll
      for (int k = 0; k < 8; ++k) { g[k] = 0.f; up[k] = 0.f; }
#define FDOT(n, Q) { const unsigned ob = dppu<Q>(xn); unsigned w[8]; \
      pack_pair(w, gw[2 * (n)], gw[2 * (n) + 1]); _Pragma("unroll") for (int k = 0; k < 8; ++k) g[k] = dot2(w[k], ob, g[k]); \
      pack_pair(w, uw[2 * (n)], uw[2 * (n) + 1]); _Pragma("unroll") for (int k = 0; k < 8; ++k) up[k] = dot2(w[k], ob, up[k]); }
      FDOT(0, 0x00) FDOT(1, 0x55) FDOT(2, 0xAA) FDOT(3, 0xFF)
      float a8[8], a4[4];
#pragma unroll
      for (int k = 0; k < 8; ++k) a8[k] = swap32_add(g[k], up[k]);
#pragma unroll
      for (int k = 0; k < 4; ++k) { float t = swap16_add(a8[k], a8[k + 4]); t += dpp<ROR8>(t); t += dpp<ROR4>(t); a4[k] = t; }
      const int sel = (lane >> 2) & 3;
      const float v = sel == 0 ? a4[0] : sel == 1 ? a4[1] : sel == 2 ? a4[2] : a4[3];
      putf(G + G_A + (cu * 4 + kf) * 64 + lane, tag, v);
    }
    if (!NO_FFN && j >= 21) {
      const int fg = j - 21, kg = lane >> 3, cc = lane & 7;
      unsigned wp[8][8];
      {
        const auto rw = __builtin_amdgcn_make_buffer_rsrc((void*)(wd + (size_t)fg * (128 * D * 2) + xg * 128), 0, (F - fg * 128) * (D * 2) - xg * 128, 0x00020000);
        const unsigned o = (32 * (kg >> 2) + 4 * ((kg >> 1) & 1) + 2 * (kg & 1)) * (D * 2) + cc * 16;
        u32x4 wv[16];
#pragma unroll
        for (int n = 0; n < 8; ++n) {
          wv[2 * n] = __builtin_amdgcn_raw_buffer_load_b128(rw, o + (8 * (n & 3) + (n >> 2)) * (D * 2), 0, 2);
          wv[2 * n + 1] = __builtin_amdgcn_raw_buffer_load_b128(rw, o + (64 + 8 * (n & 3) + (n >> 2)) * (D * 2), 0, 2);
        }
#pragma unroll
        for (int n = 0; n < 8; ++n) pack_pair(wp[n], wv[2 * n], wv[2 * n + 1]);
      }
      unsigned av[12], aw[4];
      sweep<12, 16>(G + G_A + fg * 1024 + lane, 64, tag, av);
      sweep<4, 16>(G + G_A + fg * 1024 + (fg == 10 ? 512 : 768) + lane, 64, tag, aw);
      float v[4];
#pragma unroll
      for (int i = 0; i < 3; ++i) v[i] = (__builtin_bit_cast(float, av[4 * i]) + __builtin_bit_cast(float, av[4 * i + 1])) + (__builtin_bit_cast(float, av[4 * i + 2]) + __builtin_bit_cast(float, av[4 * i + 3]));
      v[3] = (__builtin_bit_cast(float, aw[0]) + __builtin_bit_cast(float, aw[1])) + (__builtin_bit_cast(float, aw[2]) + __builtin_bit_cast(float, aw[3]));
      float hv[2];
#pragma unroll
      for (int i = 0; i < 2; ++i) {
        auto sw = __builtin_amdgcn_permlane32_swap(__float_as_uint(v[2 * i]), __float_as_uint(v[2 * i + 1]), false, false);
        const float gg = rbf(__uint_as_float(sw[0])), uu = rbf(__uint_as_float(sw[1]));
        const float inner = 0.7978845608028654f * (gg + 0.044715f * gg * gg * gg);
        hv[i] = rbf(0.5f * gg * (1.0f + tanhf(inner))) * uu;
      }
      const float yv = gemv64(wp, pk_bf16(hv[0], hv[1]), lane);
      putf(G + G_Y + (xg * YROWS + H + fg) * 64 + lane, tag, yv);
    }
    STAMP(1);
    if (j < 28) {
      LDS_BARRIER();
      FLUSH();
    } else {
      const int qd = j - 28, hg = lane >> 4, li = qd * 16 + (lane & 15), col = xg * 64 + (li & 7) * 8 + (li >> 3);
      gu64* yg = G + G_Y + (xg * YROWS) * 64 + li;
      const float xo = bf_lo(__builtin_amdgcn_raw_buffer_load_b16(rx, col * 2, 0, 0));
      float fs = 0.f;
      if (!NO_FFN) {
        unsigned fv[2], fw[1];
        sweep<2, 16>(yg + (H + hg) * 64, 256, tag, fv);
        sweep<1, 16>(yg + (H + 8 + (hg == 3 ? 0 : hg)) * 64, 0, tag, fw);
        fs = (__builtin_bit_cast(float, fv[0]) + __builtin_bit_cast(float, fv[1])) + (hg == 3 ? 0.f : __builtin_bit_cast(float, fw[0]));
      }
      fs = rbf(xrow_sum(fs));
      LDS_BARRIER();
      unsigned yv[8];
      sweep<8, 1>(yg + hg * 64, 256, tag, yv);
      STAMP(4);
      float a = 0.f;
#pragma unroll
      for (int i = 0; i < 8; ++i) a += __builtin_bit_cast(float, yv[i]);
      a = rbf(xrow_sum(a));
      const float o = rbf(xo + a) + fs;
      if (lane < 16) out[col] = (uint16_t)pk_bf16(o, o);
      STAMP(5);
      FLUSH();
    }
    ROLE_END();
  }

  {
    const int wi = wave - 2, r = lane >> 4, c = lane & 15;
#define kr ar
#define vr br
#define ISSUE_K(slot, st) _Pragma("unroll") for (int t_ = 0; t_ < 4; ++t_) kr[slot][t_] = __builtin_amdgcn_raw_buffer_load_b128(ra, voff + t_ * 1024, (st) * STEP_BYTES, 2)
#define ISSUE_V(slot, st) _Pragma("unroll") for (int t_ = 0; t_ < 4; ++t_) vr[slot][t_] = __builtin_amdgcn_raw_buffer_load_b128(rb, voff + t_ * 1024, (st) * STEP_BYTES, 2)
    const int tw = (wi - LEAD) * SLOT;
    unsigned tlast = tstart;
    unsigned torg = tstart;
#define DUE(n) (torg + (unsigned)((tw + (n) * (WORKERS * SLOT)) >> 8))
#pragma unroll
    for (int g = 1; g < RING; ++g) {
      GATE(DUE(2 * g)); ISSUE_K(g, g);
      GATE(DUE(2 * g + 1)); ISSUE_V(g, g);
    }
    STAMP(7);
    LDS_BARRIER();
    const u32x4 qp = *reinterpret_cast<const u32x4*>(lds + L_Q + c * 16);
    torg = __builtin_amdgcn_readfirstlane(*reinterpret_cast<const unsigned*>(lds + L_T0));
    STAMP(1);
    float m = -INFINITY, l = 0.f, acc[8];
#pragma unroll
    for (int k = 0; k < 8; ++k) acc[k] = 0.f;

#define PIN_ACC() asm volatile("" : "+v"(acc[0]), "+v"(acc[1]), "+v"(acc[2]), "+v"(acc[3]), "+v"(acc[4]), "+v"(acc[5]), "+v"(acc[6]), "+v"(acc[7]), "+v"(l), "+v"(m))
#define PIN_S() asm volatile("" : "+v"(s[0]), "+v"(s[1]), "+v"(s[2]), "+v"(s[3]))
#define TILE(slot, st, ISSUE) do { \
    float s[4]; \
    _Pragma("unroll") for (int t = 0; t < 4; ++t) { \
      float d = dot2(kr[slot][t][0], qp[0], 0.f); d = dot2(kr[slot][t][1], qp[1], d); d = dot2(kr[slot][t][2], qp[2], d); s[t] = dot2(kr[slot][t][3], qp[3], d); } \
    __builtin_amdgcn_sched_barrier(0); \
    if (ISSUE) { PIN_S(); GATE(DUE(2 * (st))); ISSUE_K(slot, st); } \
    __builtin_amdgcn_sched_barrier(0); \
    _Pragma("unroll") for (int t = 0; t < 4; ++t) s[t] = row16_sum(s[t]); \
    { const unsigned p01 = pk_bf16(s[0], s[1]), p23 = pk_bf16(s[2], s[3]); \
      const unsigned r01 = pk_bf16(bf_lo(p01) * SCALE, bf_hi(p01) * SCALE), r23 = pk_bf16(bf_lo(p23) * SCALE, bf_hi(p23) * SCALE); \
      s[0] = bf_lo(r01); s[1] = bf_hi(r01); s[2] = bf_lo(r23); s[3] = bf_hi(r23); } \
    const float mn = fmaxf(fmaxf(fmaxf(s[0], s[1]), fmaxf(s[2], s[3])), m); \
    const float nb = -mn * LOG2E; \
    const float al = __builtin_amdgcn_exp2f(fmaf(m, LOG2E, nb)); \
    m = mn; \
    float p[4]; \
    _Pragma("unroll") for (int t = 0; t < 4; ++t) p[t] = __builtin_amdgcn_exp2f(fmaf(s[t], LOG2E, nb)); \
    l = fmaf(l, al, (p[0] + p[1]) + (p[2] + p[3])); \
    _Pragma("unroll") for (int k = 0; k < 8; ++k) acc[k] *= al; \
    _Pragma("unroll") for (int t = 0; t < 4; ++t) \
      _Pragma("unroll") for (int k = 0; k < 4; ++k) { acc[2 * k] = fmaf(p[t], bf_lo(vr[slot][t][k]), acc[2 * k]); acc[2 * k + 1] = fmaf(p[t], bf_hi(vr[slot][t][k]), acc[2 * k + 1]); } \
    __builtin_amdgcn_sched_barrier(0); \
    if (ISSUE) { PIN_ACC(); GATE(DUE(2 * (st) + 1)); ISSUE_V(slot, st); } \
    __builtin_amdgcn_sched_barrier(0); \
  } while (0)

    static_assert(RING == 3 && STEPS % 3 == 2, "loop and tail below");
#pragma unroll 1
    for (int st = RING; st + 2 < STEPS; st += 3) {
      TILE(0, st, true);
      TILE(1, st + 1, true);
      TILE(2, st + 2, true);
    }
    TILE(0, STEPS - 2, true);
    TILE(1, STEPS - 1, true);
    TILE(2, 0, false);
    TILE(0, 0, false);
    TILE(1, 0, false);
    STAMP(2);
    const float mw = xrow_max(m);
    const float sc = __builtin_amdgcn_exp2f((m - mw) * LOG2E);
    l = xrow_sum(l * sc);
    float a4[4], a2[2];
#pragma unroll
    for (int k = 0; k < 4; ++k) a4[k] = swap32_add(acc[k] * sc, acc[k + 4] * sc);
#pragma unroll
    for (int k = 0; k < 2; ++k) a2[k] = swap16_add(a4[k], a4[k + 2]);
    *reinterpret_cast<f32x2*>(lds + L_OP + wi * 512 + c * 32 + r * 8) = f32x2{a2[0], a2[1]};
    if (lane == 0) *reinterpret_cast<f32x2*>(lds + L_ML + wi * 8) = f32x2{mw, l};
    LDS_BARRIER();
    FLUSH();
#undef kr
#undef vr
  }

}

}

extern "C" void kernel_launch(void* const* d_in, const int* in_sizes, int n_in, void* d_out, int out_size, void* d_ws, size_t ws_size,
                              hipStream_t stream) {
  fused_decode<<<NWG, NTHR, 0, stream>>>((const uint16_t*)d_in[0], (const char*)d_in[1], (const char*)d_in[2], (const char*)d_in[3],
                                         (const char*)d_in[4], (const char*)d_in[6], (const char*)d_in[7], (const char*)d_in[8],
                                         (char*)d_ws, (uint16_t*)d_out TUNE_PASS);
}
```

```cpp
#include <hip/hip_runtime.h>
#include <hip/hip_bf16.h>
#include <stdint.h>

#define TUNE_ARGS
#define TUNE_PASS
constexpr int SLOT = 3650, LEAD = -8, GAP = $, MODE = 48;

namespace {

constexpr int D = 512, H = 32, SK = 32768, DH = 128, F = 1376;
constexpr int NWG = 256, NSPLIT = 8, WORKERS = 8, NWAVES = WORKERS + 2, NTHR = NWAVES * 64;
constexpr int KEYS_WG = SK / NSPLIT;
constexpr int TILE_KEYS = 16;
constexpr int STEPS = KEYS_WG / (WORKERS * TILE_KEYS);
constexpr int STEP_BYTES = WORKERS * TILE_KEYS * DH * 2;
constexpr int CHUNK_BYTES = KEYS_WG * DH * 2;
constexpr int FFN_UNITS = F / 8;
constexpr int FFN_PER_X = 22;
constexpr int RING = 6;
constexpr unsigned SPIN_CAP = 1u << 20;

constexpr int G_Q = 0;
constexpr int G_M = G_Q + H * 512;
constexpr int G_P = G_M + H * 16;
constexpr int YROWS = 44;
constexpr int G_Y = G_P + NWG * 128;
constexpr int G_A = G_Y + 8 * YROWS * 64;

constexpr int L_Q = 0;
constexpr int L_ML = 256;
constexpr int L_FLAG = 320;
constexpr int L_OP = 384;
constexpr int L_SC = L_OP + 4096;
constexpr int L_END = L_SC + STEPS * WORKERS * 32;

typedef unsigned u32x4 __attribute__((ext_vector_type(4)));
typedef unsigned u32x2 __attribute__((ext_vector_type(2)));
typedef float f32x2 __attribute__((ext_vector_type(2)));
typedef __bf16 bf16x2 __attribute__((ext_vector_type(2)));
typedef __attribute__((address_space(1))) unsigned long long gu64;

extern "C" __device__ unsigned long long llvm_dispatch_id() __asm("llvm.amdgcn.dispatch.id");

__device__ __forceinline__ float dot2(unsigned a, unsigned b, float acc) {
  return __builtin_amdgcn_fdot2_f32_bf16(__builtin_bit_cast(bf16x2, a), __builtin_bit_cast(bf16x2, b), acc, false);
}
__device__ __forceinline__ unsigned pk_bf16(float lo, float hi) {
  f32x2 v = {lo, hi};
  return __builtin_bit_cast(unsigned, __builtin_convertvector(v, bf16x2));
}
__device__ __forceinline__ float asf(unsigned x) { return __builtin_bit_cast(float, x); }
__device__ __forceinline__ float bf_lo(unsigned x) { return asf(x << 16); }
__device__ __forceinline__ float bf_hi(unsigned x) { return asf(x & 0xffff0000u); }
__device__ __forceinline__ float rbf(float x) { return bf_lo(pk_bf16(x, x)); }

template <int CTRL> __device__ __forceinline__ unsigned dppu(unsigned x) { return (unsigned)__builtin_amdgcn_mov_dpp((int)x, CTRL, 0xf, 0xf, true); }
template <int CTRL> __device__ __forceinline__ float dpp(float x) { return asf(dppu<CTRL>(__builtin_bit_cast(unsigned, x))); }
constexpr int XOR1 = 0xB1, XOR2 = 0x4E, HMIR = 0x141, ROR4 = 0x124, ROR8 = 0x128;
__device__ __forceinline__ float row16_sum(float x) {
  x += dpp<XOR1>(x); x += dpp<XOR2>(x); x += dpp<HMIR>(x); x += dpp<ROR8>(x); return x;
}
__device__ __forceinline__ float oct_sum(float x) { x += dpp<XOR1>(x); x += dpp<XOR2>(x); x += dpp<HMIR>(x); return x; }
__device__ __forceinline__ float oct_max(float x) { x = fmaxf(x, dpp<XOR1>(x)); x = fmaxf(x, dpp<XOR2>(x)); x = fmaxf(x, dpp<HMIR>(x)); return x; }
__device__ __forceinline__ float swap16_add(float a, float b) {
  auto s = __builtin_amdgcn_permlane16_swap(__float_as_uint(a), __float_as_uint(b), false, false);
  return __uint_as_float(s[0]) + __uint_as_float(s[1]);
}
__device__ __forceinline__ float swap32_add(float a, float b) {
  auto s = __builtin_amdgcn_permlane32_swap(__float_as_uint(a), __float_as_uint(b), false, false);
  return __uint_as_float(s[0]) + __uint_as_float(s[1]);
}
__device__ __forceinline__ float xrow_sum(float x) { x = swap16_add(x, x); return swap32_add(x, x); }
__device__ __forceinline__ float xrow_max(float x) {
  auto s = __builtin_amdgcn_permlane16_swap(__float_as_uint(x), __float_as_uint(x), false, false);
  x = fmaxf(__uint_as_float(s[0]), __uint_as_float(s[1]));
  auto t = __builtin_amdgcn_permlane32_swap(__float_as_uint(x), __float_as_uint(x), false, false);
  return fmaxf(__uint_as_float(t[0]), __uint_as_float(t[1]));
}
__device__ __forceinline__ float wave_sum(float x) { return xrow_sum(row16_sum(x)); }
__device__ __forceinline__ float rdlane(float x, int l) { return asf(__builtin_amdgcn_readlane(__builtin_bit_cast(int, x), l)); }

#define PUTSCOPE __HIP_MEMORY_SCOPE_AGENT
__device__ __forceinline__ void put(gu64* g, unsigned tag, unsigned v) {
  __hip_atomic_store(g, ((unsigned long long)tag << 32) | v, __ATOMIC_RELAXED, PUTSCOPE);
}
__device__ __forceinline__ void putf(gu64* g, unsigned tag, float v) { put(g, tag, __builtin_bit_cast(unsigned, v)); }
#define GETSCOPE __HIP_MEMORY_SCOPE_AGENT
__device__ __forceinline__ unsigned long long get(gu64* g) { return __hip_atomic_load(g, __ATOMIC_RELAXED, GETSCOPE); }
template <int N, int SLEEP> __device__ __forceinline__ void sweep(gu64* g, int S, unsigned tag, unsigned (&v)[N], unsigned cap = SPIN_CAP) {
#pragma unroll 1
  for (unsigned spins = 0; spins < cap; ++spins) {
    bool ok = true;
#pragma unroll
    for (int k = 0; k < N; ++k) { const unsigned long long x = get(g + k * S); v[k] = (unsigned)x; ok &= (unsigned)(x >> 32) == tag; }
    if (__all(ok)) return;
    if (SLEEP) __builtin_amdgcn_s_sleep(SLEEP);
  }
}

#define STAMP(i) do {} while (0)
#define FLUSH() do {} while (0)
#define LDS_BARRIER() do { __builtin_amdgcn_sched_barrier(0); asm volatile("s_waitcnt lgkmcnt(0)\n\ts_barrier" ::: "memory"); __builtin_amdgcn_sched_barrier(0); } while (0)
#define END_WAVE() asm volatile("s_endpgm" ::: "memory")

#define WAIT_UNTIL(due) asm volatile("1:\n\ts_memrealtime vcc\n\ts_waitcnt lgkmcnt(0)\n\ts_sub_i32 vcc_lo, vcc_lo, %0\n\ts_cmp_ge_i32 vcc_lo, 0\n\ts_cbranch_scc1 2f\n\ts_sleep 4\n\ts_branch 1b\n2:" \
                 :: "s"(due) : "vcc", "scc", "memory")
#define GATE(due) do { __builtin_amdgcn_sched_barrier(0); \
    asm volatile("s_waitcnt vmcnt(0)\n" \
                 "1:\n\ts_memrealtime vcc\n\ts_waitcnt lgkmcnt(0)\n\ts_sub_i32 %0, vcc_lo, %1\n\ts_cmp_ge_i32 %0, 0\n\ts_cbranch_scc1 2f\n\ts_sleep 1\n\ts_branch 1b\n2:" \
                 : "=&s"(late) : "s"(due) : "vcc", "scc", "memory"); \
    __builtin_amdgcn_sched_barrier(0); } while (0)

__device__ __forceinline__ void ln_stats(u32x4 xv, float& mean_b, float& den_b) {
  float e[8];
#pragma unroll
  for (int j = 0; j < 4; ++j) { e[2 * j] = bf_lo(xv[j]); e[2 * j + 1] = bf_hi(xv[j]); }
  float s = 0.f;
#pragma unroll
  for (int j = 0; j < 8; ++j) s += e[j];
  const float mean = wave_sum(s) * (1.0f / D);
  float v = 0.f;
#pragma unroll
  for (int j = 0; j < 8; ++j) { const float d = e[j] - mean; v = fmaf(d, d, v); }
  const float var = wave_sum(v) * (1.0f / D);
  mean_b = rbf(mean);
  den_b = rbf(sqrtf(rbf(rbf(var) + 1e-5f)));
}
__device__ __forceinline__ float ln_apply(float x, float mean_b, float den_b) { return rbf(rbf(x - mean_b) / den_b); }
__device__ __forceinline__ unsigned ln_pair(u32x4 xv, unsigned xp) {
  float mean_b, den_b;
  ln_stats(xv, mean_b, den_b);
  return pk_bf16(ln_apply(bf_lo(xp), mean_b, den_b), ln_apply(bf_hi(xp), mean_b, den_b));
}

__device__ __forceinline__ unsigned launch_tag() {
  const unsigned long long id = llvm_dispatch_id();
  unsigned qh = (unsigned)((unsigned long long)__builtin_amdgcn_dispatch_ptr() >> 22);
  qh *= 0x9E3779B1u; qh ^= qh >> 15;
  const unsigned t = (unsigned)id + qh;
  return ((t << 2) & 0xFFFFFE00u) | ((t << 1) & 0x000000FEu) | 1u;
}

constexpr float SCALE = 0.08838834764831845f, LOG2E = 1.4426950408889634f;

__device__ __forceinline__ float gemv64(const unsigned (&wp)[8][8], unsigned vp, int lane) {
  const unsigned x4 = dppu<0x1B>(dppu<HMIR>(vp));
  const unsigned lo = (lane & 4) ? x4 : vp, hi = (lane & 4) ? vp : x4;
  float y[8];
#pragma unroll
  for (int k = 0; k < 8; ++k) y[k] = 0.f;
#define ODOT(n, src, Q) { const unsigned ob = dppu<Q>(src); _Pragma("unroll") for (int k = 0; k < 8; ++k) y[k] = dot2(wp[n][k], ob, y[k]); }
  ODOT(0, lo, 0x00) ODOT(1, lo, 0x55) ODOT(2, lo, 0xAA) ODOT(3, lo, 0xFF)
  ODOT(4, hi, 0x00) ODOT(5, hi, 0x55) ODOT(6, hi, 0xAA) ODOT(7, hi, 0xFF)
#undef ODOT
  float a4[4], a2[2];
#pragma unroll
  for (int k = 0; k < 4; ++k) a4[k] = swap32_add(y[k], y[k + 4]);
#pragma unroll
  for (int k = 0; k < 2; ++k) a2[k] = swap16_add(a4[k], a4[k + 2]);
  const float t0 = a2[0] + dpp<ROR8>(a2[0]), t1 = a2[1] + dpp<ROR8>(a2[1]);
  return (lane & 8) ? t1 : t0;
}
__device__ __forceinline__ void pack_pair(unsigned (&w)[8], u32x4 r0, u32x4 r1) {
#pragma unroll
  for (int k = 0; k < 4; ++k) {
    w[2 * k] = __builtin_amdgcn_perm(r1[k], r0[k], 0x05040100u);
    w[2 * k + 1] = __builtin_amdgcn_perm(r1[k], r0[k], 0x07060302u);
  }
}

__global__ void __launch_bounds__(NTHR) fused_decode(const uint16_t* x, const char* kc, const char* vc, const char* wq, const char* __restrict__ wo,
                                                      const char* __restrict__ wg, const char* __restrict__ wu, const char* __restrict__ wd,
                                                      char* ws, uint16_t* out TUNE_ARGS) {
  __shared__ __attribute__((aligned(16))) char lds[L_END];
  const int tid = threadIdx.x, lane = tid & 63, wave = __builtin_amdgcn_readfirstlane(tid >> 6);
  const int b = blockIdx.x, xg = b & 7, j = b >> 3;
  const int h = (j >> 3) * 8 + ((xg + 3 * (j >> 3)) & 7), sp = j & 7;
  STAMP(0);
  const unsigned tstart = (unsigned)__builtin_amdgcn_s_memrealtime();
  asm volatile("" : "+s"(x), "+s"(kc), "+s"(vc), "+s"(wq));
  volatile __attribute__((address_space(3))) unsigned* flag = (volatile __attribute__((address_space(3))) unsigned*)(lds + L_FLAG);
  const bool isw = wave >= 2;
  const unsigned offa = isw ? (unsigned)(h * NSPLIT + sp) * CHUNK_BYTES : (sp & 3) * (128 * H * DH * 2) + h * 256 + (sp >> 2) * 128;
  const unsigned nrec = wave == 1 ? 0 : isw ? CHUNK_BYTES : 127 * H * DH * 2 + 128;
  const unsigned s1 = isw ? 1024 : H * DH * 2;
  const unsigned voff = isw ? (wave - 2) * (TILE_KEYS * DH * 2) + lane * 16 : (lane >> 3) * (16 * H * DH * 2) + (lane & 7) * 16;
  const auto rx = __builtin_amdgcn_make_buffer_rsrc((void*)x, 0, isw ? 0 : D * 2, 0x00020000);
  u32x4 xv = __builtin_amdgcn_raw_buffer_load_b128(rx, lane * 16, 0, 0);
  const auto rk = __builtin_amdgcn_make_buffer_rsrc((void*)((isw ? kc : wq) + offa), 0, nrec, 0x00020000);
  u32x4 buf[RING][4];
  __builtin_amdgcn_sched_barrier(0);
#pragma unroll
  for (int t = 0; t < 4; ++t) buf[0][t] = __builtin_amdgcn_raw_buffer_load_b128(rk, voff, t * s1, 2);
  __builtin_amdgcn_sched_barrier(0);
#define ROLE_END() do { END_WAVE(); \
    _Pragma("unroll") for (int t_ = 0; t_ < 4; ++t_) asm volatile("" : "=v"(buf[0][t_])); \
    asm volatile("" : "=v"(xv)); } while (0)
  int role = wave;

  if (role == 0) {
    const int kg = lane >> 3, cc = lane & 7;
    const unsigned xp = __builtin_amdgcn_raw_buffer_load_b32(rx, lane * 4, (sp & 3) * 256, 0);
    u32x4 wv[12];
#pragma unroll
    for (int n = 0; n < 12; ++n) wv[n] = __builtin_amdgcn_raw_buffer_load_b128(rk, voff, (4 + n) * (H * DH * 2), 2);
    __builtin_amdgcn_sched_barrier(0);
    const auto rw = __builtin_amdgcn_make_buffer_rsrc((void*)(wo + (size_t)h * (DH * D * 2) + sp * 128), 0, DH * D * 2 - sp * 128, 0x00020000);
    u32x4 wo_[16];
    __builtin_amdgcn_s_setprio(3);
    gu64* G = (gu64*)ws;
    const unsigned tag = launch_tag();
    put(G + G_Q + h * 512 + sp * 64 + lane, tag ^ 0x100u, 0u);
#pragma unroll
    for (int n = 0; n < 16; ++n) wo_[n] = __builtin_amdgcn_raw_buffer_load_b128(rw, kg * 16 * (D * 2) + cc * 16, (MODE & 1) ? n * (D * 2) : 0x40000000, 2);
    __builtin_amdgcn_sched_barrier(0);
    STAMP(7);
    unsigned wp[8][8];
    {
      unsigned xn = ln_pair(xv, xp);
      asm volatile("" : "+v"(xn));
      __builtin_amdgcn_sched_barrier(0);
      pack_pair(wp[0], buf[0][0], buf[0][1]); pack_pair(wp[1], buf[0][2], buf[0][3]);
#pragma unroll
      for (int n = 0; n < 6; ++n) pack_pair(wp[2 + n], wv[2 * n], wv[2 * n + 1]);
      putf(G + G_Q + h * 512 + sp * 64 + lane, tag, gemv64(wp, xn, lane));
    }
    STAMP(6);
    if (MODE & 32) {
#pragma unroll
      for (int n = 0; n < 16; ++n) wo_[n] = __builtin_amdgcn_raw_buffer_load_b128(rw, kg * 16 * (D * 2) + cc * 16, n * (D * 2), 2);
    }
    {
      unsigned qv[8];
      __builtin_amdgcn_s_sleep(16);
      sweep<8, 8>(G + G_Q + h * 512 + lane, 64, tag, qv);
      const unsigned qq = pk_bf16((asf(qv[0]) + asf(qv[1])) + (asf(qv[2]) + asf(qv[3])), (asf(qv[4]) + asf(qv[5])) + (asf(qv[6]) + asf(qv[7])));
      *reinterpret_cast<uint16_t*>(lds + L_Q + (cc * 8 + kg) * 2) = (uint16_t)qq;
      *reinterpret_cast<uint16_t*>(lds + L_Q + 128 + (cc * 8 + kg) * 2) = (uint16_t)(qq >> 16);
      asm volatile("s_waitcnt lgkmcnt(0)" ::: "memory");
      *flag = tag;
    }
    STAMP(1);
    {
      if (!(MODE & 41)) {
#pragma unroll
        for (int n = 0; n < 16; ++n) wo_[n] = __builtin_amdgcn_raw_buffer_load_b128(rw, kg * 16 * (D * 2) + cc * 16, n * (D * 2), 2);
      }
#pragma unroll
      for (int n = 0; n < 8; ++n) pack_pair(wp[n], wo_[2 * n], wo_[2 * n + 1]);
    }
#pragma unroll
    for (int n = 0; n < 8; ++n)
#pragma unroll
      for (int k = 0; k < 8; ++k) asm volatile("" : "+v"(wp[n][k]));
    LDS_BARRIER();
    STAMP(2);
    {
      const f32x2 ml = *reinterpret_cast<const f32x2*>(lds + L_ML + (lane & 7) * 8);
      f32x2 ov[8];
#pragma unroll
      for (int w = 0; w < 8; ++w) ov[w] = *reinterpret_cast<const f32x2*>(lds + L_OP + w * 512 + lane * 8);
      const float M = oct_max(ml[0]);
      const float sw = __builtin_amdgcn_exp2f((ml[0] - M) * LOG2E);
      const float L = oct_sum(ml[1] * sw);
      float o0 = 0.f, o1 = 0.f;
#pragma unroll
      for (int w = 0; w < 8; ++w) { const float s_ = rdlane(sw, w); o0 = fmaf(s_, ov[w][0], o0); o1 = fmaf(s_, ov[w][1], o1); }
      gu64* pg = G + G_P + (h * NSPLIT + sp) * 128;
      putf(pg + lane, tag, o0);
      putf(pg + 64 + lane, tag, o1);
      if (lane < 2) putf(G + G_M + h * 16 + lane * 8 + sp, tag, lane ? L : M);
    }
    STAMP(3);
    {
      unsigned pv[18];
      gu64* pg = G + G_P + h * (NSPLIT * 128) + lane;
      gu64* mg = G + G_M + h * 16 + (lane & 7);
      for (int i = 0; i < (GAP & 255); ++i) __builtin_amdgcn_s_sleep(1);
#pragma unroll 1
      for (unsigned spins = 0; spins < SPIN_CAP; ++spins) {
        bool ok = true;
#pragma unroll
        for (int k = 0; k < 18; ++k) {
          const unsigned long long v = get(k < 16 ? pg + k * 64 : mg + (k - 16) * 8);
          pv[k] = (unsigned)v; ok &= (unsigned)(v >> 32) == tag;
        }
        if (__all(ok)) break;
        __builtin_amdgcn_s_sleep(2);
      }
      STAMP(4);
      const float ms = asf(pv[16]), ls = asf(pv[17]);
      const float M = oct_max(ms);
      const float sw = __builtin_amdgcn_exp2f((ms - M) * LOG2E);
      const float L = oct_sum(ls * sw);
      const float wgt = sw / L;
      float o0 = 0.f, o1 = 0.f;
#pragma unroll
      for (int s = 0; s < 8; ++s) { const float s_ = rdlane(wgt, s); o0 = fmaf(s_, asf(pv[2 * s]), o0); o1 = fmaf(s_, asf(pv[2 * s + 1]), o1); }
      putf(G + G_Y + (sp * YROWS + h) * 64 + lane, tag, gemv64(wp, pk_bf16(o0, o1), lane));
      STAMP(5);
    }
    FLUSH();
    ROLE_END();
  }

  asm volatile("" : "+s"(role));
  if (role >= 2) {
    const int wi = wave - 2, r = lane >> 4, c = lane & 15;
    const auto rv = __builtin_amdgcn_make_buffer_rsrc((void*)(vc + offa), 0, CHUNK_BYTES, 0x00020000);
    const int tw = (wi - LEAD) * SLOT;
#define DUE(n) (tstart + (unsigned)((tw + (n) * (WORKERS * SLOT)) >> 8))
    int late = 0;
#define ISSUE(slot, rs, n, st) do { GATE(DUE(n)); \
      _Pragma("unroll") for (int t = 0; t < 4; ++t) buf[slot][t] = __builtin_amdgcn_raw_buffer_load_b128(rs, voff + t * 1024, (st) * STEP_BYTES, 2); \
      __builtin_amdgcn_sched_barrier(0); } while (0)
#pragma unroll
    for (int g = 1; g < RING; ++g) ISSUE(g, rk, g, g);
    STAMP(7);
    {
      const unsigned tag = launch_tag();
      for (unsigned spins = 0; *flag != tag && spins < (SPIN_CAP << 4); ++spins) __builtin_amdgcn_s_sleep(1);
      asm volatile("" ::: "memory");
    }
    const u32x4 qp = *reinterpret_cast<const u32x4*>(lds + L_Q + c * 16);
    STAMP(1);
    char* scw = lds + L_SC + wi * 32 + r * 8;
#define KDOT(slot) \
      float s[4]; \
      _Pragma("unroll") for (int t = 0; t < 4; ++t) { \
        float d = dot2(buf[slot][t][0], qp[0], 0.f); d = dot2(buf[slot][t][1], qp[1], d); d = dot2(buf[slot][t][2], qp[2], d); s[t] = dot2(buf[slot][t][3], qp[3], d); } \
      asm volatile("" : "+v"(s[0]), "+v"(s[1]), "+v"(s[2]), "+v"(s[3]));
#define KFIN(st) \
      _Pragma("unroll") for (int t = 0; t < 4; ++t) s[t] = row16_sum(s[t]); \
      { const unsigned p01 = pk_bf16(s[0], s[1]), p23 = pk_bf16(s[2], s[3]); \
        const u32x2 sv = {pk_bf16(bf_lo(p01) * SCALE, bf_hi(p01) * SCALE), pk_bf16(bf_lo(p23) * SCALE, bf_hi(p23) * SCALE)}; \
        if (c == 0) *reinterpret_cast<u32x2*>(scw + (st) * (WORKERS * 32)) = sv; } \
      __builtin_amdgcn_sched_barrier(0);
    { KDOT(4) ISSUE(4, rk, RING, RING); KFIN(4) }
    { KDOT(0) KFIN(0) } { KDOT(1) KFIN(1) }
    { KDOT(5) ISSUE(5, rk, RING + 1, RING + 1); KFIN(5) }
    { KDOT(2) KFIN(2) } { KDOT(3) KFIN(3) }
#pragma unroll 1
    for (int st = RING; st < STEPS - 2; st += 2) {
      { KDOT(4) ISSUE(4, rk, st + 2, st + 2); KFIN(st) }
      { KDOT(5) ISSUE(5, rk, st + 3, st + 3); KFIN(st + 1) }
    }
    { KDOT(4) ISSUE(4, rv, STEPS, 0); KFIN(STEPS - 2) }
    { KDOT(5) ISSUE(5, rv, STEPS + 1, 1); KFIN(STEPS - 1) }

    float m = -INFINITY, l = 0.f, acc[8];
#pragma unroll
    for (int k = 0; k < 8; ++k) acc[k] = 0.f;
#define VTILE(slot, st) do { \
      const u32x2 sv = *reinterpret_cast<const u32x2*>(scw + (st) * (WORKERS * 32)); \
      const float s0 = bf_lo(sv[0]), s1_ = bf_hi(sv[0]), s2 = bf_lo(sv[1]), s3 = bf_hi(sv[1]); \
      const float mn = fmaxf(fmaxf(fmaxf(s0, s1_), fmaxf(s2, s3)), m); \
      const float nb = -mn * LOG2E; \
      const float al = __builtin_amdgcn_exp2f(fmaf(m, LOG2E, nb)); \
      m = mn; \
      const float p[4] = {__builtin_amdgcn_exp2f(fmaf(s0, LOG2E, nb)), __builtin_amdgcn_exp2f(fmaf(s1_, LOG2E, nb)), __builtin_amdgcn_exp2f(fmaf(s2, LOG2E, nb)), __builtin_amdgcn_exp2f(fmaf(s3, LOG2E, nb))}; \
      l = fmaf(l, al, (p[0] + p[1]) + (p[2] + p[3])); \
      _Pragma("unroll") for (int k = 0; k < 8; ++k) acc[k] *= al; \
      _Pragma("unroll") for (int t = 0; t < 4; ++t) \
        _Pragma("unroll") for (int k = 0; k < 4; ++k) { acc[2 * k] = fmaf(p[t], bf_lo(buf[slot][t][k]), acc[2 * k]); acc[2 * k + 1] = fmaf(p[t], bf_hi(buf[slot][t][k]), acc[2 * k + 1]); } \
      asm volatile("" : "+v"(acc[0]), "+v"(acc[1]), "+v"(acc[2]), "+v"(acc[3]), "+v"(acc[4]), "+v"(acc[5]), "+v"(acc[6]), "+v"(acc[7]), "+v"(l), "+v"(m)); \
    } while (0)
#pragma unroll 1
    for (int st = 0; st < STEPS - 2; st += 2) {
      VTILE(4, st); ISSUE(4, rv, STEPS + st + 2, st + 2);
      VTILE(5, st + 1); ISSUE(5, rv, STEPS + st + 3, st + 3);
    }
    VTILE(4, STEPS - 2);
    VTILE(5, STEPS - 1);
    STAMP(2);
    const float mw = xrow_max(m);
    const float sc = __builtin_amdgcn_exp2f((m - mw) * LOG2E);
    l = xrow_sum(l * sc);
    float a4[4], a2[2];
#pragma unroll
    for (int k = 0; k < 4; ++k) a4[k] = swap32_add(acc[k] * sc, acc[k + 4] * sc);
#pragma unroll
    for (int k = 0; k < 2; ++k) a2[k] = swap16_add(a4[k], a4[k + 2]);
    *reinterpret_cast<f32x2*>(lds + L_OP + wi * 512 + c * 32 + r * 8) = f32x2{a2[0], a2[1]};
    if (lane == 0) *reinterpret_cast<f32x2*>(lds + L_ML + wi * 8) = f32x2{mw, l};
    LDS_BARRIER();
    FLUSH();
    ROLE_END();
  }

  __builtin_amdgcn_s_setprio(2);
  const unsigned tag = launch_tag();
  gu64* G = (gu64*)ws;
  const int u = xg * FFN_PER_X + j;
  if (MODE & 16) {
    const unsigned nt_ = tag ^ 0x100u;
    put(G + G_P + (h * NSPLIT + sp) * 128 + lane, nt_, 0u);
    put(G + G_P + (h * NSPLIT + sp) * 128 + 64 + lane, nt_, 0u);
    put(G + G_Y + (sp * YROWS + h) * 64 + lane, nt_, 0u);
    if (lane < 2) put(G + G_M + h * 16 + lane * 8 + sp, nt_, 0u);
    if (j < FFN_PER_X && u < FFN_UNITS) put(G + G_A + ((u % 43) * 4 + u / 43) * 64 + lane, nt_, 0u);
    if (j >= 21) put(G + G_Y + (xg * YROWS + H + j - 21) * 64 + lane, nt_, 0u);
  }
  WAIT_UNTIL(tstart + (GAP >> 16));
  if (!(MODE & 4) && j < FFN_PER_X && u < FFN_UNITS) {
    const int kf = u / 43, cu = u - kf * 43;
    const unsigned xp = __builtin_amdgcn_raw_buffer_load_b32(rx, lane * 4, kf * 256, 0);
    u32x4 gw[8], uw[8];
    {
      const unsigned o = kf * (128 * F * 2) + cu * 64, n = 128 * F * 2 - cu * 64;
      const auto rg_ = __builtin_amdgcn_make_buffer_rsrc((void*)(wg + o), 0, n, 0x00020000);
      const auto ru_ = __builtin_amdgcn_make_buffer_rsrc((void*)(wu + o), 0, n, 0x00020000);
      const unsigned vo = (lane >> 2) * (8 * F * 2) + (lane & 3) * 16;
#pragma unroll
      for (int i = 0; i < 8; ++i) {
        gw[i] = __builtin_amdgcn_raw_buffer_load_b128(rg_, vo, i * (F * 2), 2);
        uw[i] = __builtin_amdgcn_raw_buffer_load_b128(ru_, vo, i * (F * 2), 2);
        __builtin_amdgcn_sched_barrier(0);
      }
    }
    const unsigned xn = ln_pair(xv, xp);
    float g[8], up[8];
#pragma unroll
    for (int k = 0; k < 8; ++k) { g[k] = 0.f; up[k] = 0.f; }
#define FDOT(n, Q) { const unsigned ob = dppu<Q>(xn); unsigned w[8]; \
      pack_pair(w, gw[2 * (n)], gw[2 * (n) + 1]); _Pragma("unroll") for (int k = 0; k < 8; ++k) g[k] = dot2(w[k], ob, g[k]); \
      pack_pair(w, uw[2 * (n)], uw[2 * (n) + 1]); _Pragma("unroll") for (int k = 0; k < 8; ++k) up[k] = dot2(w[k], ob, up[k]); }
    FDOT(0, 0x00) FDOT(1, 0x55) FDOT(2, 0xAA) FDOT(3, 0xFF)
    float a8[8], a4[4];
#pragma unroll
    for (int k = 0; k < 8; ++k) a8[k] = swap32_add(g[k], up[k]);
#pragma unroll
    for (int k = 0; k < 4; ++k) { float t = swap16_add(a8[k], a8[k + 4]); t += dpp<ROR8>(t); t += dpp<ROR4>(t); a4[k] = t; }
    const int sel = (lane >> 2) & 3;
    const float v = sel == 0 ? a4[0] : sel == 1 ? a4[1] : sel == 2 ? a4[2] : a4[3];
    putf(G + G_A + (cu * 4 + kf) * 64 + lane, tag, v);
  }
  if (!(MODE & 4) && j >= 21) {
    const int fg = j - 21, kg = lane >> 3, cc = lane & 7;
    unsigned wp[8][8];
    {
      const auto rw = __builtin_amdgcn_make_buffer_rsrc((void*)(wd + (size_t)fg * (128 * D * 2) + xg * 128), 0, (F - fg * 128) * (D * 2) - xg * 128, 0x00020000);
      const unsigned o = (32 * (kg >> 2) + 4 * ((kg >> 1) & 1) + 2 * (kg & 1)) * (D * 2) + cc * 16;
      u32x4 wv[16];
#pragma unroll
      for (int n = 0; n < 8; ++n) {
        wv[2 * n] = __builtin_amdgcn_raw_buffer_load_b128(rw, o + (8 * (n & 3) + (n >> 2)) * (D * 2), 0, 2);
        wv[2 * n + 1] = __builtin_amdgcn_raw_buffer_load_b128(rw, o + (64 + 8 * (n & 3) + (n >> 2)) * (D * 2), 0, 2);
        __builtin_amdgcn_sched_barrier(0);
      }
#pragma unroll
      for (int n = 0; n < 8; ++n) pack_pair(wp[n], wv[2 * n], wv[2 * n + 1]);
    }
    WAIT_UNTIL(tstart + 1000);
    unsigned av[12], aw[4];
    sweep<12, 32>(G + G_A + fg * 1024 + lane, 64, tag, av);
    sweep<4, 32>(G + G_A + fg * 1024 + (fg == 10 ? 512 : 768) + lane, 64, tag, aw);
    float v[4];
#pragma unroll
    for (int i = 0; i < 3; ++i) v[i] = (asf(av[4 * i]) + asf(av[4 * i + 1])) + (asf(av[4 * i + 2]) + asf(av[4 * i + 3]));
    v[3] = (asf(aw[0]) + asf(aw[1])) + (asf(aw[2]) + asf(aw[3]));
    float hv[2];
#pragma unroll
    for (int i = 0; i < 2; ++i) {
      auto sw = __builtin_amdgcn_permlane32_swap(__float_as_uint(v[2 * i]), __float_as_uint(v[2 * i + 1]), false, false);
      const float gg = rbf(__uint_as_float(sw[0])), uu = rbf(__uint_as_float(sw[1]));
      const float inner = 0.7978845608028654f * (gg + 0.044715f * gg * gg * gg);
      hv[i] = rbf(0.5f * gg * (1.0f + tanhf(inner))) * uu;
    }
    putf(G + G_Y + (xg * YROWS + H + fg) * 64 + lane, tag, gemv64(wp, pk_bf16(hv[0], hv[1]), lane));
  }
  {
    WAIT_UNTIL(tstart + 3000);
    const auto rv = __builtin_amdgcn_make_buffer_rsrc((void*)(vc + (size_t)(h * NSPLIT + sp) * CHUNK_BYTES), 0, CHUNK_BYTES, 0x00020000);
    const unsigned t_ = __builtin_amdgcn_raw_buffer_load_b32(rv, 0, 0, 2);
    asm volatile("s_waitcnt vmcnt(0)" :: "v"(t_) : "memory");
  }
  STAMP(1);
  if (j < 28) {
    LDS_BARRIER();
  } else {
    const int qd = j - 28, hg = lane >> 4, li = qd * 16 + (lane & 15), col = xg * 64 + (li & 7) * 8 + (li >> 3);
    gu64* yg = G + G_Y + (xg * YROWS) * 64 + li;
    const float xo = bf_lo(__builtin_amdgcn_raw_buffer_load_b16(rx, col * 2, 0, 0));
    unsigned fv[2], fw[1];
    sweep<2, 32>(yg + (H + hg) * 64, 256, tag, fv, (MODE & 4) ? 1u : SPIN_CAP);
    sweep<1, 32>(yg + (H + 8 + (hg == 3 ? 0 : hg)) * 64, 0, tag, fw, (MODE & 4) ? 1u : SPIN_CAP);
    const float fs = rbf(xrow_sum((asf(fv[0]) + asf(fv[1])) + (hg == 3 ? 0.f : asf(fw[0]))));
    LDS_BARRIER();
    for (int i = 0; i < ((GAP >> 8) & 255); ++i) __builtin_amdgcn_s_sleep(1);
    unsigned yv[8];
    sweep<8, 1>(yg + hg * 64, 256, tag, yv);
    STAMP(4);
    float a = 0.f;
#pragma unroll
    for (int i = 0; i < 8; ++i) a += asf(yv[i]);
    a = rbf(xrow_sum(a));
    const float o = rbf(xo + a) + fs;
    if (lane < 16) out[col] = (uint16_t)pk_bf16(o, o);
    STAMP(5);
  }
  FLUSH();
}

}

extern "C" void kernel_launch(void* const* d_in, const int* in_sizes, int n_in, void* d_out, int out_size, void* d_ws, size_t ws_size,
                              hipStream_t stream) {
  fused_decode<<<NWG, NTHR, 0, stream>>>((const uint16_t*)d_in[0], (const char*)d_in[1], (const char*)d_in[2], (const char*)d_in[3],
                                         (const char*)d_in[4], (const char*)d_in[6], (const char*)d_in[7], (const char*)d_in[8],
                                         (char*)d_ws, (uint16_t*)d_out TUNE_PASS);
}
```

```cpp
#include <hip/hip_runtime.h>
#include <hip/hip_bf16.h>
#include <stdint.h>

namespace {

constexpr int D = 512, H = 32, DH = 128, F = 1376;
constexpr int NWG = 256, NUNIT = 8, WORKERS = 8, NWAVES = WORKERS + 3, NTHR = NWAVES * 64;
constexpr int STEPS = 32;
constexpr int PAGE_BYTES = 2 << 20;
constexpr int STEP_BYTES = PAGE_BYTES / STEPS;
constexpr int FFN_UNITS = F / 8;
constexpr int FFN_PER_X = 22;
constexpr int RING = 6;
constexpr int S1 = 20;
constexpr int XB = 6;

constexpr int SLOT_MIX = 3600, SLOT_NEAR = 3465, SLOT_XCHG = 25, LEAD = 15;
constexpr int T_TOUCH = 100, T_FFN_A = 200, T_PROBE0 = 700, T_PROBE1 = 1700, T_DECIDE = 2000, T_FFN_B = 2800, T_VTOUCH = 3300, T_FSUM = 4000;
constexpr int NCK = 11, T_CK0 = 1100, T_CKI = 500, CK_TOL = 30;
constexpr int NPMIN = 4, NPMAX = 8;
constexpr int SLEEP_P = 13, SLEEP_Y = 12;
constexpr unsigned GIVE_UP = 30000000u;

constexpr int G_Q = 0;
constexpr int G_M = G_Q + H * 512;
constexpr int G_P = G_M + H * 16;
constexpr int YROWS = 44;
constexpr int G_Y = G_P + NWG * 128;
constexpr int G_A = G_Y + 8 * YROWS * 64;
constexpr int G_C = G_A + 43 * 256;
constexpr int G_L = G_C + 64;
constexpr int G_END = G_L + NCK * 64;
constexpr size_t WS_PAIR = (size_t)1 << 20;
static_assert((size_t)G_END * 8 <= WS_PAIR, "workspace layout");

constexpr int L_Q = 0;
constexpr int L_F = 256;
constexpr int L_W = 288;
constexpr int L_OP = 384;
constexpr int L_SC = L_OP + 4096;
constexpr int L_END = L_SC + 16384;

typedef unsigned u32x4 __attribute__((ext_vector_type(4)));
typedef unsigned u32x2 __attribute__((ext_vector_type(2)));
typedef float f32x2 __attribute__((ext_vector_type(2)));
typedef __bf16 bf16x2 __attribute__((ext_vector_type(2)));
typedef __attribute__((address_space(1))) unsigned long long gu64;

extern "C" __device__ unsigned long long llvm_dispatch_id() __asm("llvm.amdgcn.dispatch.id");

__device__ __forceinline__ float dot2(unsigned a, unsigned b, float acc) {
  return __builtin_amdgcn_fdot2_f32_bf16(__builtin_bit_cast(bf16x2, a), __builtin_bit_cast(bf16x2, b), acc, false);
}
__device__ __forceinline__ unsigned pk_bf16(float lo, float hi) {
  f32x2 v = {lo, hi};
  return __builtin_bit_cast(unsigned, __builtin_convertvector(v, bf16x2));
}
__device__ __forceinline__ float asf(unsigned x) { return __builtin_bit_cast(float, x); }
__device__ __forceinline__ float bf_lo(unsigned x) { return asf(x << 16); }
__device__ __forceinline__ float bf_hi(unsigned x) { return asf(x & 0xffff0000u); }
__device__ __forceinline__ float rbf(float x) { return bf_lo(pk_bf16(x, x)); }

template <int CTRL> __device__ __forceinline__ unsigned dppu(unsigned x) { return (unsigned)__builtin_amdgcn_mov_dpp((int)x, CTRL, 0xf, 0xf, true); }
template <int CTRL> __device__ __forceinline__ float dpp(float x) { return asf(dppu<CTRL>(__builtin_bit_cast(unsigned, x))); }
constexpr int XOR1 = 0xB1, XOR2 = 0x4E, HMIR = 0x141, ROR4 = 0x124, ROR8 = 0x128;
__device__ __forceinline__ float row16_sum(float x) {
  x += dpp<XOR1>(x); x += dpp<XOR2>(x); x += dpp<HMIR>(x); x += dpp<ROR8>(x); return x;
}
__device__ __forceinline__ float oct_sum(float x) { x += dpp<XOR1>(x); x += dpp<XOR2>(x); x += dpp<HMIR>(x); return x; }
__device__ __forceinline__ float oct_max(float x) { x = fmaxf(x, dpp<XOR1>(x)); x = fmaxf(x, dpp<XOR2>(x)); x = fmaxf(x, dpp<HMIR>(x)); return x; }
__device__ __forceinline__ float swap16_add(float a, float b) {
  auto s = __builtin_amdgcn_permlane16_swap(__float_as_uint(a), __float_as_uint(b), false, false);
  return __uint_as_float(s[0]) + __uint_as_float(s[1]);
}
__device__ __forceinline__ float swap32_add(float a, float b) {
  auto s = __builtin_amdgcn_permlane32_swap(__float_as_uint(a), __float_as_uint(b), false, false);
  return __uint_as_float(s[0]) + __uint_as_float(s[1]);
}
__device__ __forceinline__ float xrow_sum(float x) { x = swap16_add(x, x); return swap32_add(x, x); }
__device__ __forceinline__ float xrow_max(float x) {
  auto s = __builtin_amdgcn_permlane16_swap(__float_as_uint(x), __float_as_uint(x), false, false);
  x = fmaxf(__uint_as_float(s[0]), __uint_as_float(s[1]));
  auto t = __builtin_amdgcn_permlane32_swap(__float_as_uint(x), __float_as_uint(x), false, false);
  return fmaxf(__uint_as_float(t[0]), __uint_as_float(t[1]));
}
__device__ __forceinline__ float wave_sum(float x) { return xrow_sum(row16_sum(x)); }

__device__ __forceinline__ unsigned now() { return (unsigned)__builtin_amdgcn_s_memrealtime(); }
__device__ __forceinline__ bool expired(unsigned tstart) { return now() - tstart > GIVE_UP; }
__device__ __forceinline__ void put(gu64* g, unsigned tag, unsigned v) {
  __hip_atomic_store(g, ((unsigned long long)tag << 32) | v, __ATOMIC_RELAXED, __HIP_MEMORY_SCOPE_AGENT);
}
__device__ __forceinline__ void putf(gu64* g, unsigned tag, float v) { put(g, tag, __builtin_bit_cast(unsigned, v)); }
__device__ __forceinline__ unsigned long long get(gu64* g) { return __hip_atomic_load(g, __ATOMIC_RELAXED, __HIP_MEMORY_SCOPE_AGENT); }
__device__ __forceinline__ unsigned long long flag_of(unsigned tag, unsigned bits) { return ((unsigned long long)tag << 32) | (~tag & ~3u) | bits; }
__device__ __forceinline__ bool flag_ok(unsigned long long x, unsigned tag) { return (x | 3ull) == (flag_of(tag, 3u)); }
template <int N, int SLEEP> __device__ __forceinline__ void sweep(gu64* g, int S, unsigned tag, unsigned (&v)[N], unsigned tstart) {
  for (;;) {
    bool ok = true;
#pragma unroll
    for (int k = 0; k < N; ++k) { const unsigned long long x = get(g + k * S); v[k] = (unsigned)x; ok &= (unsigned)(x >> 32) == tag; }
    if (__all(ok) || expired(tstart)) return;
    __builtin_amdgcn_s_sleep(SLEEP);
  }
}
__device__ __forceinline__ unsigned wait_flag(gu64* g, unsigned tag, unsigned tstart) {
  for (;;) {
    const unsigned long long x = get(g);
    if (__all(flag_ok(x, tag)) || expired(tstart)) return (unsigned)x & 3u;
    __builtin_amdgcn_s_sleep(16);
  }
}

#define LDS_BARRIER() do { __builtin_amdgcn_sched_barrier(0); asm volatile("s_waitcnt lgkmcnt(0)\n\ts_barrier" ::: "memory"); __builtin_amdgcn_sched_barrier(0); } while (0)
#define WAIT_UNTIL(due) asm volatile("1:\n\ts_memrealtime vcc\n\ts_waitcnt lgkmcnt(0)\n\ts_sub_i32 vcc_lo, vcc_lo, %0\n\ts_cmp_ge_i32 vcc_lo, 0\n\ts_cbranch_scc1 2f\n\ts_sleep 4\n\ts_branch 1b\n2:" \
                 :: "s"(due) : "vcc", "scc", "memory")
#define GATE(due) do { __builtin_amdgcn_sched_barrier(0); \
    asm volatile("s_waitcnt vmcnt(0)\n" \
                 "1:\n\ts_memrealtime vcc\n\ts_waitcnt lgkmcnt(0)\n\ts_sub_i32 %0, vcc_lo, %1\n\ts_cmp_ge_i32 %0, 0\n\ts_cbranch_scc1 2f\n\ts_sleep 1\n\ts_branch 1b\n2:" \
                 : "=&s"(late) : "s"(due) : "vcc", "scc", "memory"); \
    __builtin_amdgcn_sched_barrier(0); } while (0)

__device__ __forceinline__ unsigned probe(const char* a) {
  unsigned v; unsigned long long t0, t1;
  asm volatile("s_waitcnt lgkmcnt(0)\n\ts_memtime %0\n\ts_waitcnt lgkmcnt(0)\n\ts_load_dword %2, %3, 0x0\n\ts_waitcnt lgkmcnt(0)\n\ts_memtime %1\n\ts_waitcnt lgkmcnt(0)"
               : "=&s"(t0), "=&s"(t1), "=&s"(v) : "s"(a) : "memory");
  return (unsigned)(t1 - t0);
}
__device__ __forceinline__ unsigned near_parity(const char* group, unsigned salt, unsigned deadline, unsigned f) {
  unsigned m0 = ~0u, m1 = ~0u;
  const char* a = group + ((salt >> 1) & 31) * 128 + ((salt >> 10) & 15) * 65536 + (size_t)((salt >> 16) & 6) * PAGE_BYTES;
  const char* b = group + ((salt >> 5) & 31) * 128 + ((salt >> 13) & 15) * 65536 + (size_t)(((salt >> 19) & 6) + 1) * PAGE_BYTES + 8192;
  probe(a + 49152); probe(b + 49152);
#pragma unroll 1
  for (int i = 0; i < NPMAX; ++i) {
    const unsigned o = (i & 2) * 2048 + (i >> 2) * 16384;
    const char* p = ((i & 1) ? b : a) + o;
    m0 = min(m0, probe(p)); m1 = min(m1, probe((i & 1) ? p - 8192 : p + 8192));
    if (i + 1 >= NPMIN && (m0 > m1 ? m0 - m1 : m1 - m0) >= 5 * f) break;
    if ((int)(now() - deadline) > 0) { if (i + 1 < NPMIN) return 2u; break; }
  }
  if (min(m0, m1) < 17 * f || (m0 > m1 ? m0 - m1 : m1 - m0) < 4 * f) return 2u;
  return m0 < m1 ? 0u : 1u;
}

__device__ __forceinline__ void ln_stats(u32x4 xv, float& mean_b, float& den_b) {
  float e[8];
#pragma unroll
  for (int j = 0; j < 4; ++j) { e[2 * j] = bf_lo(xv[j]); e[2 * j + 1] = bf_hi(xv[j]); }
  float s = 0.f;
#pragma unroll
  for (int j = 0; j < 8; ++j) s += e[j];
  const float mean = wave_sum(s) * (1.0f / D);
  float v = 0.f;
#pragma unroll
  for (int j = 0; j < 8; ++j) { const float d = e[j] - mean; v = fmaf(d, d, v); }
  const float var = wave_sum(v) * (1.0f / D);
  mean_b = rbf(mean);
  den_b = rbf(sqrtf(rbf(rbf(var) + 1e-5f)));
}
__device__ __forceinline__ float ln_apply(float x, float mean_b, float den_b) { return rbf(rbf(x - mean_b) / den_b); }
__device__ __forceinline__ unsigned ln_pair(u32x4 xv, unsigned xp) {
  float mean_b, den_b;
  ln_stats(xv, mean_b, den_b);
  return pk_bf16(ln_apply(bf_lo(xp), mean_b, den_b), ln_apply(bf_hi(xp), mean_b, den_b));
}

__device__ __forceinline__ unsigned launch_tag() {
  const unsigned long long id = llvm_dispatch_id();
  unsigned qh = (unsigned)((unsigned long long)__builtin_amdgcn_dispatch_ptr() >> 22);
  qh *= 0x9E3779B1u; qh ^= qh >> 15;
  const unsigned t = (unsigned)id + qh;
  return ((t << 2) & 0xFFFFFE00u) | ((t << 1) & 0x000000FEu) | 1u;
}

constexpr float SCALE = 0.08838834764831845f, LOG2E = 1.4426950408889634f;

__device__ __forceinline__ float gemv64(const unsigned (&wp)[8][8], unsigned vp, int lane) {
  const unsigned x4 = dppu<0x1B>(dppu<HMIR>(vp));
  const unsigned lo = (lane & 4) ? x4 : vp, hi = (lane & 4) ? vp : x4;
  float y[8];
#pragma unroll
  for (int k = 0; k < 8; ++k) y[k] = 0.f;
#define ODOT(n, src, Q) { const unsigned ob = dppu<Q>(src); _Pragma("unroll") for (int k = 0; k < 8; ++k) y[k] = dot2(wp[n][k], ob, y[k]); }
  ODOT(0, lo, 0x00) ODOT(1, lo, 0x55) ODOT(2, lo, 0xAA) ODOT(3, lo, 0xFF)
  ODOT(4, hi, 0x00) ODOT(5, hi, 0x55) ODOT(6, hi, 0xAA) ODOT(7, hi, 0xFF)
#undef ODOT
  float a4[4], a2[2];
#pragma unroll
  for (int k = 0; k < 4; ++k) a4[k] = swap32_add(y[k], y[k + 4]);
#pragma unroll
  for (int k = 0; k < 2; ++k) a2[k] = swap16_add(a4[k], a4[k + 2]);
  const float t0 = a2[0] + dpp<ROR8>(a2[0]), t1 = a2[1] + dpp<ROR8>(a2[1]);
  return (lane & 8) ? t1 : t0;
}
__device__ __forceinline__ void pack_pair(unsigned (&w)[8], u32x4 r0, u32x4 r1) {
#pragma unroll
  for (int k = 0; k < 4; ++k) {
    w[2 * k] = __builtin_amdgcn_perm(r1[k], r0[k], 0x05040100u);
    w[2 * k + 1] = __builtin_amdgcn_perm(r1[k], r0[k], 0x07060302u);
  }
}

__global__ void __launch_bounds__(NTHR, 3) fused_decode(const uint16_t* x, const char* kc, const char* vc, const char* wq, const char* __restrict__ wo,
                                                         const char* __restrict__ wg, const char* __restrict__ wu, const char* __restrict__ wd,
                                                         char* ws, uint16_t* out) {
  __shared__ __attribute__((aligned(16))) char lds[L_END];
  const int tid = threadIdx.x, lane = tid & 63, wave = __builtin_amdgcn_readfirstlane(tid >> 6);
  const int b = blockIdx.x, xg = b & 7, j = b >> 3, side = xg >> 2;
  const int page = ((j >> 3) * 4 + ((xg + (j >> 3)) & 3)) * 8 + (j & 7);
  const int h = page >> 2, sp = (page & 3) * 2 + side;
  const unsigned tstart = now();
  asm volatile("" : "+s"(x), "+s"(kc), "+s"(vc), "+s"(wq));
  volatile __attribute__((address_space(3))) unsigned* lw = (volatile __attribute__((address_space(3))) unsigned*)(lds + L_W);
  volatile __attribute__((address_space(3))) unsigned long long* lf = (volatile __attribute__((address_space(3))) unsigned long long*)(lds + L_F);
  const bool isw = wave >= 2;
  const unsigned offa = isw ? (unsigned)page * PAGE_BYTES : (sp & 3) * (128 * H * DH * 2) + h * 256 + (sp >> 2) * 128;
  const unsigned nrec = (wave == 1 || wave == NWAVES - 1) ? 0 : isw ? PAGE_BYTES : 127 * H * DH * 2 + 128;
  const unsigned s1 = isw ? 1024 : H * DH * 2;
  unsigned voff = isw ? ((wave - 2) >> 1) * 16384 + side * 8192 + (wave & 1) * 4096 + lane * 16 : (lane >> 3) * (16 * H * DH * 2) + (lane & 7) * 16;
  const auto rx = __builtin_amdgcn_make_buffer_rsrc((void*)x, 0, isw ? 0 : D * 2, 0x00020000);
  u32x4 xv = __builtin_amdgcn_raw_buffer_load_b128(rx, lane * 16, 0, 0);
  const auto rk = __builtin_amdgcn_make_buffer_rsrc((void*)((isw ? kc : wq) + offa), 0, nrec, 0x00020000);
  u32x4 buf[RING][4];
  __builtin_amdgcn_sched_barrier(0);
#pragma unroll
  for (int t = 0; t < 4; ++t) buf[0][t] = __builtin_amdgcn_raw_buffer_load_b128(rk, voff, t * s1, 2);
  __builtin_amdgcn_sched_barrier(0);
  const unsigned tag = launch_tag();
  gu64* G = (gu64*)ws;
  char* pblk = ws + WS_PAIR + (size_t)page * 32768;
  gu64* dmy = (gu64*)(pblk + side * 16384 + 8192);
  gu64* dpe = (gu64*)(pblk + (side ^ 1) * 16384 + 8192);
#define LDS_WAIT(i) ({ unsigned long long w_; while (!flag_ok(w_ = lf[i], tag) && !expired(tstart)) __builtin_amdgcn_s_sleep(1); \
    asm volatile("" ::: "memory"); (unsigned)__builtin_amdgcn_readfirstlane((unsigned)w_ & 3u); })
#define LDS_SIGNAL(i, bits) do { asm volatile("s_waitcnt lgkmcnt(0)" ::: "memory"); lf[i] = flag_of(tag, bits); } while (0)
#define ROLE_END() do { asm volatile("s_endpgm" ::: "memory"); \
    _Pragma("unroll") for (int t_ = 0; t_ < 4; ++t_) asm volatile("" : "=v"(buf[0][t_])); \
    asm volatile("" : "=v"(xv)); } while (0)
  int role = wave;

  if (role == 0) {
    const int kg = lane >> 3, cc = lane & 7;
    const unsigned xp = __builtin_amdgcn_raw_buffer_load_b32(rx, lane * 4, (sp & 3) * 256, 0);
    u32x4 wv[16];
#pragma unroll
    for (int n = 0; n < 12; ++n) wv[n] = __builtin_amdgcn_raw_buffer_load_b128(rk, voff, (4 + n) * (H * DH * 2), 2);
    __builtin_amdgcn_sched_barrier(0);
    __builtin_amdgcn_s_setprio(3);
    const auto rs = __builtin_amdgcn_make_buffer_rsrc((void*)pblk, 0, 32768, 0x00020000);
    put(G + G_Q + h * 512 + sp * 64 + lane, tag ^ 0x100u, 0u);
    unsigned wp[8][8];
    {
      unsigned xn = ln_pair(xv, xp);
      asm volatile("" : "+v"(xn));
      __builtin_amdgcn_sched_barrier(0);
      pack_pair(wp[0], buf[0][0], buf[0][1]); pack_pair(wp[1], buf[0][2], buf[0][3]);
#pragma unroll
      for (int n = 0; n < 6; ++n) pack_pair(wp[2 + n], wv[2 * n], wv[2 * n + 1]);
      putf(G + G_Q + h * 512 + sp * 64 + lane, tag, gemv64(wp, xn, lane));
    }
    __builtin_amdgcn_sched_barrier(0);
    {
      const auto rw = __builtin_amdgcn_make_buffer_rsrc((void*)(wo + (size_t)h * (DH * D * 2) + sp * 128), 0, DH * D * 2 - sp * 128, 0x00020000);
#pragma unroll
      for (int n = 0; n < 16; ++n) wv[n] = __builtin_amdgcn_raw_buffer_load_b128(rw, kg * 16 * (D * 2) + cc * 16, n * (D * 2), 2);
    }
    __builtin_amdgcn_sched_barrier(0);
    {
      unsigned qv[8];
      __builtin_amdgcn_s_sleep(16);
      sweep<8, 8>(G + G_Q + h * 512 + lane, 64, tag, qv, tstart);
      const unsigned qq = pk_bf16((asf(qv[0]) + asf(qv[1])) + (asf(qv[2]) + asf(qv[3])), (asf(qv[4]) + asf(qv[5])) + (asf(qv[6]) + asf(qv[7])));
      *reinterpret_cast<uint16_t*>(lds + L_Q + (cc * 8 + kg) * 2) = (uint16_t)qq;
      *reinterpret_cast<uint16_t*>(lds + L_Q + 128 + (cc * 8 + kg) * 2) = (uint16_t)(qq >> 16);
      if (lane < 24) lw[lane] = 0;
      LDS_SIGNAL(0, 0u);
    }
#pragma unroll
    for (int n = 0; n < 8; ++n) pack_pair(wp[n], wv[2 * n], wv[2 * n + 1]);
#pragma unroll
    for (int n = 0; n < 8; ++n)
#pragma unroll
      for (int k = 0; k < 8; ++k) asm volatile("" : "+v"(wp[n][k]));
    const unsigned flipk = LDS_WAIT(1) & 1u, pv = LDS_WAIT(2) & 1u;
    const bool x0 = (unsigned)side != pv, x1 = (side ^ flipk) != pv;
#pragma unroll
    for (int e = 0; e < 2; ++e) {
      const int g0 = e ? XB : 0, g1 = e ? 8 : XB;
      while (!__all(lw[8 + (lane & 7)] >= (unsigned)(4 * g1)) && !expired(tstart)) __builtin_amdgcn_s_sleep(8);
      asm volatile("" ::: "memory");
      if (x1 || (x0 && e == 0)) {
#pragma unroll
        for (int g = g0; g < g1; ++g)
          if (g * 4 < S1 ? x0 : x1)
            __builtin_amdgcn_raw_buffer_store_b128(*reinterpret_cast<const u32x4*>(lds + L_SC + g * 1024 + lane * 16), rs, side * 16384 + g * 1024 + lane * 16, 0, 18);
        asm volatile("s_waitcnt vmcnt(0)" ::: "memory");
        if (lane == 0) __hip_atomic_store(dmy + e, flag_of(tag, 0u), __ATOMIC_RELAXED, __HIP_MEMORY_SCOPE_AGENT);
        __builtin_amdgcn_s_sleep(24);
        (void)wait_flag(dpe + e, tag, tstart);
        asm volatile("" ::: "memory");
        u32x4 v[XB];
#pragma unroll
        for (int g = g0; g < g1; ++g)
          v[g - g0] = __builtin_amdgcn_raw_buffer_load_b128(rs, (side ^ 1) * 16384 + g * 1024 + lane * 16, (g * 4 < S1 ? x0 : x1) ? 0 : 0x40000000, 18);
#pragma unroll
        for (int g = g0; g < g1; ++g) *reinterpret_cast<u32x4*>(lds + L_SC + 8192 + g * 1024 + lane * 16) = v[g - g0];
        asm volatile("s_waitcnt lgkmcnt(0)" ::: "memory");
      }
      lw[0] = g1;
    }
    const int us = (sp & 6) + pv;
    {
      float sc[64];
#pragma unroll
      for (int g = 0; g < 8; ++g) {
        const u32x4 v = *reinterpret_cast<const u32x4*>(lds + L_SC + ((g * 4 < S1 ? x0 : x1) ? 8192 : 0) + g * 1024 + lane * 16);
#pragma unroll
        for (int k = 0; k < 4; ++k) { sc[g * 8 + 2 * k] = bf_lo(v[k]); sc[g * 8 + 2 * k + 1] = bf_hi(v[k]); }
      }
      float mx = sc[0];
#pragma unroll
      for (int k = 1; k < 64; ++k) mx = fmaxf(mx, sc[k]);
      mx = oct_max(mx); mx = fmaxf(mx, dpp<ROR8>(mx)); mx = xrow_max(mx);
      const float nb = -mx * LOG2E;
      float sm = 0.f;
#pragma unroll
      for (int k = 0; k < 64; ++k) sm += __builtin_amdgcn_exp2f(fmaf(sc[k], LOG2E, nb));
      sm = wave_sum(sm);
      if (lane < 2) putf(G + G_M + h * 16 + lane * 8 + us, tag, lane ? sm : mx);
      __builtin_amdgcn_s_sleep(64);
      unsigned mv[2];
      sweep<2, 32>(G + G_M + h * 16 + (lane & 7), 8, tag, mv, tstart);
      const float M = oct_max(asf(mv[0]));
      const float L = oct_sum(asf(mv[1]) * __builtin_amdgcn_exp2f((asf(mv[0]) - M) * LOG2E));
      lw[2] = __builtin_bit_cast(unsigned, M);
      lw[3] = __builtin_bit_cast(unsigned, 1.0f / L);
      LDS_SIGNAL(3, 0u);
    }
    gu64* pw = G + G_P + (h * NUNIT + us) * 128 + lane;
    gu64* pg = G + G_P + h * (NUNIT * 128) + lane;
    gu64* yw = G + G_Y + (sp * YROWS + h) * 64 + lane;
    asm volatile("" : "+v"(pw), "+v"(pg), "+v"(yw));
    LDS_BARRIER();
    {
      f32x2 ov[8];
#pragma unroll
      for (int w = 0; w < 8; ++w) ov[w] = *reinterpret_cast<const f32x2*>(lds + L_OP + w * 512 + lane * 8);
      const f32x2 o = ((ov[0] + ov[1]) + (ov[2] + ov[3])) + ((ov[4] + ov[5]) + (ov[6] + ov[7]));
      putf(pw, tag, o[0]);
      putf(pw + 64, tag, o[1]);
    }
    {
      unsigned pvv[16];
      __builtin_amdgcn_s_sleep(SLEEP_P);
      sweep<16, 2>(pg, 64, tag, pvv, tstart);
      const float o0 = ((asf(pvv[0]) + asf(pvv[2])) + (asf(pvv[4]) + asf(pvv[6]))) + ((asf(pvv[8]) + asf(pvv[10])) + (asf(pvv[12]) + asf(pvv[14])));
      const float o1 = ((asf(pvv[1]) + asf(pvv[3])) + (asf(pvv[5]) + asf(pvv[7]))) + ((asf(pvv[9]) + asf(pvv[11])) + (asf(pvv[13]) + asf(pvv[15])));
      putf(yw, tag, gemv64(wp, pk_bf16(o0, o1), lane));
      lw[4] = 1;
    }
    ROLE_END();
  }

  asm volatile("" : "+s"(role));
  if (role == NWAVES - 1) {
    if ((j & 7) == 0) {
      unsigned half;
      asm volatile("s_getreg_b32 %0, hwreg(HW_REG_XCC_ID, 2, 1)" : "=s"(half));
      WAIT_UNTIL(tstart + T_PROBE0 - 200);
      const unsigned c0 = (unsigned)__builtin_amdgcn_s_memtime(), r0 = now();
      WAIT_UNTIL(r0 + 200);
      const unsigned f = ((unsigned)__builtin_amdgcn_s_memtime() - c0) / (now() - r0);
      const unsigned d = near_parity((side ? vc : kc + PAGE_BYTES / 2) + (size_t)page * PAGE_BYTES, tag * 0x9E3779B1u >> 8, tstart + T_PROBE1, f);
      if (lane == 0) __hip_atomic_store(G + G_C + side * 16 + (page >> 3), flag_of(tag, d ^ half), __ATOMIC_RELAXED, __HIP_MEMORY_SCOPE_AGENT);
    }
    (void)LDS_WAIT(0);
    int shift = 0;
#pragma unroll 1
    for (int ck = 0; ck < NCK; ++ck) {
      const unsigned tc = tstart + (shift >> 8) + T_CK0 + ck * T_CKI;
      if ((j & 6) == 2) {
        WAIT_UNTIL(tc);
        int lt = min((int)lw[16 + (lane & 7)], 2000);
        lt += (int)dppu<XOR1>(lt); lt += (int)dppu<XOR2>(lt); lt += (int)dppu<HMIR>(lt);
        if (lane == 0) put(G + G_L + ck * 64 + (j >> 3) * 16 + (j & 1) * 8 + xg, tag, (tstart << 16) | lt);
      }
      WAIT_UNTIL(tc + 220);
      unsigned v[1];
      sweep<1, 32>(G + G_L + ck * 64 + lane, 0, tag, v, tstart);
      if (ck == 0) shift = (int)(wave_sum((float)((int)(v[0] - (tstart << 16)) >> 16)) * 4.f);
      const int mean = (int)(wave_sum((float)(v[0] & 0xffffu)) * 0.5f);
      shift += max(mean - CK_TOL * 256, 0) >> 1;
      shift = __builtin_amdgcn_readfirstlane(shift);
      lw[1] = shift;
    }
    LDS_BARRIER();
    ROLE_END();
  }

  asm volatile("" : "+s"(role));
  if (role >= 2) {
    const int wi = wave - 2, r = lane >> 4, c = lane & 15;
    const auto rv = __builtin_amdgcn_make_buffer_rsrc((void*)(vc + offa), 0, PAGE_BYTES, 0x00020000);
    int inc = WORKERS * SLOT_MIX, due = (wi + LEAD) * SLOT_MIX, late = 0, shift = 0;
#define ISSUE(slot, rs, st) do { due += inc; GATE(tstart + (unsigned)((due + shift) >> 8)); \
      _Pragma("unroll") for (int t = 0; t < 4; ++t) buf[slot][t] = __builtin_amdgcn_raw_buffer_load_b128(rs, voff + t * 1024, (st) * STEP_BYTES, 2); \
      __builtin_amdgcn_sched_barrier(0); } while (0)
#pragma unroll
    for (int g = 1; g < RING; ++g) ISSUE(g, rk, g);
    (void)LDS_WAIT(0);
    const u32x4 qp = *reinterpret_cast<const u32x4*>(lds + L_Q + c * 16);
    char* scw = lds + L_SC + wi * 32 + r * 8;
#define KDOT(slot) \
      float s[4]; \
      _Pragma("unroll") for (int t = 0; t < 4; ++t) { \
        float d = dot2(buf[slot][t][0], qp[0], 0.f); d = dot2(buf[slot][t][1], qp[1], d); d = dot2(buf[slot][t][2], qp[2], d); s[t] = dot2(buf[slot][t][3], qp[3], d); } \
      asm volatile("" : "+v"(s[0]), "+v"(s[1]), "+v"(s[2]), "+v"(s[3]));
#define KFIN(st) \
      _Pragma("unroll") for (int t = 0; t < 4; ++t) s[t] = row16_sum(s[t]); \
      { const unsigned p01 = pk_bf16(s[0], s[1]), p23 = pk_bf16(s[2], s[3]); \
        const u32x2 sv = {pk_bf16(bf_lo(p01) * SCALE, bf_hi(p01) * SCALE), pk_bf16(bf_lo(p23) * SCALE, bf_hi(p23) * SCALE)}; \
        if (c == 0) *reinterpret_cast<u32x2*>(scw + (st) * (WORKERS * 32)) = sv; } \
      __builtin_amdgcn_sched_barrier(0);
#define SYNC() do { lw[16 + wi] = late; shift = __builtin_amdgcn_readfirstlane(lw[1]); } while (0)
#define PROGRESS(n) do { asm volatile("" ::: "memory"); lw[8 + wi] = (n); SYNC(); } while (0)
    { KDOT(4) ISSUE(4, rk, RING); KFIN(4) }
    { KDOT(0) KFIN(0) } { KDOT(1) KFIN(1) }
    { KDOT(5) ISSUE(5, rk, RING + 1); KFIN(5) }
    { KDOT(2) KFIN(2) } { KDOT(3) KFIN(3) }
    static_assert(RING == 6 && S1 % 4 == 0 && S1 >= RING + 2 && S1 <= 4 * XB && XB >= 4 && XB < 8, "parity switch");
    unsigned flipk = 0, dvv = 0;
    int fast = 0;
#pragma unroll 1
    for (int st = RING; st < STEPS - 2; st += 2) {
      if (st + 2 == S1) {
        dvv = LDS_WAIT(2);
        const unsigned d = LDS_WAIT(1);
        flipk = d & 1u; voff ^= flipk << 13;
        fast = WORKERS * (SLOT_NEAR + ((side ^ flipk) != (dvv & 1u) ? SLOT_XCHG : 0));
        inc = (d & 2) ? WORKERS * SLOT_MIX : fast;
      }
      { KDOT(4) ISSUE(4, rk, st + 2); KFIN(st) }
      { KDOT(5) ISSUE(5, rk, st + 3); KFIN(st + 1) }
      PROGRESS(st + 2);
    }
    const unsigned pv = dvv & 1u;
    voff = (voff & ~8192u) | (pv << 13);
    inc = (dvv & 2) ? WORKERS * SLOT_MIX : fast;
    { KDOT(4) ISSUE(4, rv, 0); KFIN(STEPS - 2) }
    { KDOT(5) ISSUE(5, rv, 1); KFIN(STEPS - 1) }
    PROGRESS(STEPS);

    float m = -INFINITY, acc[8];
#pragma unroll
    for (int k = 0; k < 8; ++k) acc[k] = 0.f;
    const char* scr = scw + ((unsigned)side != pv ? 8192 : 0);
#define VTILE(slot, st) do { \
      const u32x2 sv = *reinterpret_cast<const u32x2*>(scr + (st) * (WORKERS * 32)); \
      const float s0 = bf_lo(sv[0]), s1_ = bf_hi(sv[0]), s2 = bf_lo(sv[1]), s3 = bf_hi(sv[1]); \
      const float mn = fmaxf(fmaxf(fmaxf(s0, s1_), fmaxf(s2, s3)), m); \
      const float nb = -mn * LOG2E; \
      const float al = __builtin_amdgcn_exp2f(fmaf(m, LOG2E, nb)); \
      m = mn; \
      const float p[4] = {__builtin_amdgcn_exp2f(fmaf(s0, LOG2E, nb)), __builtin_amdgcn_exp2f(fmaf(s1_, LOG2E, nb)), __builtin_amdgcn_exp2f(fmaf(s2, LOG2E, nb)), __builtin_amdgcn_exp2f(fmaf(s3, LOG2E, nb))}; \
      _Pragma("unroll") for (int k = 0; k < 8; ++k) acc[k] *= al; \
      _Pragma("unroll") for (int t = 0; t < 4; ++t) \
        _Pragma("unroll") for (int k = 0; k < 4; ++k) { acc[2 * k] = fmaf(p[t], bf_lo(buf[slot][t][k]), acc[2 * k]); acc[2 * k + 1] = fmaf(p[t], bf_hi(buf[slot][t][k]), acc[2 * k + 1]); } \
      asm volatile("" : "+v"(acc[0]), "+v"(acc[1]), "+v"(acc[2]), "+v"(acc[3]), "+v"(acc[4]), "+v"(acc[5]), "+v"(acc[6]), "+v"(acc[7]), "+v"(m)); \
    } while (0)
#pragma unroll 1
    for (int st = 0; st < STEPS - 2; st += 2) {
      if ((st & 3) == 0) {
        while (lw[0] <= (unsigned)(st >> 2) && !expired(tstart)) __builtin_amdgcn_s_sleep(1);
        asm volatile("" ::: "memory");
        if (st == S1) scr = scw + ((side ^ flipk) != pv ? 8192 : 0);
      }
      VTILE(4, st); ISSUE(4, rv, st + 2);
      VTILE(5, st + 1); ISSUE(5, rv, st + 3);
      SYNC();
    }
    (void)LDS_WAIT(3);
    const float Mh = asf(lw[2]), rl = asf(lw[3]);
    VTILE(4, STEPS - 2);
    VTILE(5, STEPS - 1);
    const float sc = __builtin_amdgcn_exp2f((m - Mh) * LOG2E) * rl;
    float a4[4], a2[2];
#pragma unroll
    for (int k = 0; k < 4; ++k) a4[k] = swap32_add(acc[k] * sc, acc[k + 4] * sc);
#pragma unroll
    for (int k = 0; k < 2; ++k) a2[k] = swap16_add(a4[k], a4[k + 2]);
    *reinterpret_cast<f32x2*>(lds + L_OP + wi * 512 + c * 32 + r * 8) = f32x2{a2[0], a2[1]};
    LDS_BARRIER();
    ROLE_END();
  }

  __builtin_amdgcn_s_setprio(2);
  const int u = xg * FFN_PER_X + j;
  const bool ffa = j < FFN_PER_X && u < FFN_UNITS;
  const int kf = u / 43, cu = u - kf * 43;
  {
    unsigned half;
    asm volatile("s_getreg_b32 %0, hwreg(HW_REG_XCC_ID, 2, 1)" : "=s"(half));
    if (side == 0 && lane == 0) __hip_atomic_store(dmy + 2, flag_of(tag, half), __ATOMIC_RELAXED, __HIP_MEMORY_SCOPE_AGENT);
  }
  WAIT_UNTIL(tstart + T_TOUCH);
  unsigned long long touch = get(G + G_P + (h * NUNIT + sp) * 128 + lane) ^ get(G + G_P + (h * NUNIT + sp) * 128 + 64 + lane) ^ get(G + G_Y + (sp * YROWS + h) * 64 + lane) ^
                             get(lane < 16 ? G + G_M + h * 16 + lane : lane < 32 ? dmy + (lane & 15) : G + G_C + (b * 16) % (64 + NCK * 64)) ^
                             get(j >= 21 ? G + G_Y + (xg * YROWS + H + j - 21) * 64 + lane : G + G_A + (ffa ? cu * 4 + kf : 0) * 64 + lane);
  __builtin_amdgcn_sched_barrier(0);
  WAIT_UNTIL(tstart + T_FFN_A);
  u32x4 wa[16];
  if (ffa) {
    const unsigned xp = __builtin_amdgcn_raw_buffer_load_b32(rx, lane * 4, kf * 256, 0);
    {
      const unsigned o = kf * (128 * F * 2) + cu * 64, n = 128 * F * 2 - cu * 64;
      const auto rg_ = __builtin_amdgcn_make_buffer_rsrc((void*)(wg + o), 0, n, 0x00020000);
      const auto ru_ = __builtin_amdgcn_make_buffer_rsrc((void*)(wu + o), 0, n, 0x00020000);
      const unsigned vo = (lane >> 2) * (8 * F * 2) + (lane & 3) * 16;
#pragma unroll
      for (int i = 0; i < 8; ++i) {
        wa[i] = __builtin_amdgcn_raw_buffer_load_b128(rg_, vo, i * (F * 2), 2);
        wa[8 + i] = __builtin_amdgcn_raw_buffer_load_b128(ru_, vo, i * (F * 2), 2);
      }
      __builtin_amdgcn_sched_barrier(0);
    }
    const unsigned xn = ln_pair(xv, xp);
    float g[8], up[8];
#pragma unroll
    for (int k = 0; k < 8; ++k) { g[k] = 0.f; up[k] = 0.f; }
#define FDOT(n, Q) { const unsigned ob = dppu<Q>(xn); unsigned w[8]; \
      pack_pair(w, wa[2 * (n)], wa[2 * (n) + 1]); _Pragma("unroll") for (int k = 0; k < 8; ++k) g[k] = dot2(w[k], ob, g[k]); \
      pack_pair(w, wa[8 + 2 * (n)], wa[9 + 2 * (n)]); _Pragma("unroll") for (int k = 0; k < 8; ++k) up[k] = dot2(w[k], ob, up[k]); }
    FDOT(0, 0x00) FDOT(1, 0x55) FDOT(2, 0xAA) FDOT(3, 0xFF)
    float a8[8], a4[4];
#pragma unroll
    for (int k = 0; k < 8; ++k) a8[k] = swap32_add(g[k], up[k]);
#pragma unroll
    for (int k = 0; k < 4; ++k) { float t = swap16_add(a8[k], a8[k + 4]); t += dpp<ROR8>(t); t += dpp<ROR4>(t); a4[k] = t; }
    const int sel = (lane >> 2) & 3;
    const float v = sel == 0 ? a4[0] : sel == 1 ? a4[1] : sel == 2 ? a4[2] : a4[3];
    putf(G + G_A + (cu * 4 + kf) * 64 + lane, tag, v);
  }
  const int fg = j - 21;
  if (j >= 21) {
    const int kg = lane >> 3, cc = lane & 7;
    const auto rw = __builtin_amdgcn_make_buffer_rsrc((void*)(wd + (size_t)fg * (128 * D * 2) + xg * 128), 0, (F - fg * 128) * (D * 2) - xg * 128, 0x00020000);
    const unsigned o = (32 * (kg >> 2) + 4 * ((kg >> 1) & 1) + 2 * (kg & 1)) * (D * 2) + cc * 16;
#pragma unroll
    for (int n = 0; n < 8; ++n) {
      wa[2 * n] = __builtin_amdgcn_raw_buffer_load_b128(rw, o + (8 * (n & 3) + (n >> 2)) * (D * 2), 0, 2);
      wa[2 * n + 1] = __builtin_amdgcn_raw_buffer_load_b128(rw, o + (64 + 8 * (n & 3) + (n >> 2)) * (D * 2), 0, 2);
    }
  }
  asm volatile("" :: "v"(touch));
  {
    WAIT_UNTIL(tstart + T_DECIDE);
    const unsigned dv = wait_flag(lane < 32 ? G + G_C + lane : (side ? dpe : dmy) + 2, tag, tstart);
    const unsigned unk2 = (unsigned)__ballot(dv & 2), one2 = (unsigned)__ballot(dv & 1) & ~unk2, g_ = page >> 3;
#pragma unroll
    for (int t = 0; t < 2; ++t) {
      const unsigned unk = (unk2 >> (16 * t)) & 0xffffu, one = (one2 >> (16 * t)) & 0xffffu;
      const int n1 = __builtin_popcount(one), n0 = 16 - __builtin_popcount(unk) - n1;
      const unsigned c_ = (((unk >> g_) & 1) || n0 >= 3 * n1 || n1 >= 3 * n0) ? n1 > n0 : (one >> g_) & 1;
      const unsigned d_ = n0 + n1 < 8 ? 2u : c_ ^ (unsigned)__builtin_amdgcn_readlane(dv, 32) ^ (page & 1);
      lf[1 + t] = flag_of(tag, d_ ^ (t & side));
    }
  }
  WAIT_UNTIL(tstart + T_FFN_B);
  if (j >= 21) {
    unsigned wp[8][8];
#pragma unroll
    for (int n = 0; n < 8; ++n) pack_pair(wp[n], wa[2 * n], wa[2 * n + 1]);
    unsigned av[12], aw[4];
    sweep<12, 32>(G + G_A + fg * 1024 + lane, 64, tag, av, tstart);
    sweep<4, 32>(G + G_A + fg * 1024 + (fg == 10 ? 512 : 768) + lane, 64, tag, aw, tstart);
    float v[4];
#pragma unroll
    for (int i = 0; i < 3; ++i) v[i] = (asf(av[4 * i]) + asf(av[4 * i + 1])) + (asf(av[4 * i + 2]) + asf(av[4 * i + 3]));
    v[3] = (asf(aw[0]) + asf(aw[1])) + (asf(aw[2]) + asf(aw[3]));
    float hv[2];
#pragma unroll
    for (int i = 0; i < 2; ++i) {
      auto sw = __builtin_amdgcn_permlane32_swap(__float_as_uint(v[2 * i]), __float_as_uint(v[2 * i + 1]), false, false);
      const float gg = rbf(__uint_as_float(sw[0])), uu = rbf(__uint_as_float(sw[1]));
      const float inner = 0.7978845608028654f * (gg + 0.044715f * gg * gg * gg);
      hv[i] = rbf(0.5f * gg * (1.0f + tanhf(inner))) * uu;
    }
    putf(G + G_Y + (xg * YROWS + H + fg) * 64 + lane, tag, gemv64(wp, pk_bf16(hv[0], hv[1]), lane));
  }
  WAIT_UNTIL(tstart + T_VTOUCH);
  {
    const auto rv = __builtin_amdgcn_make_buffer_rsrc((void*)(vc + (size_t)page * PAGE_BYTES), 0, PAGE_BYTES, 0x00020000);
    const unsigned t_ = __builtin_amdgcn_raw_buffer_load_b32(rv, 0, 0, 2);
    asm volatile("" :: "v"(t_));
  }
  if (j < 28) {
    LDS_BARRIER();
  } else {
    const int qd = j - 28, hg = lane >> 4, li = qd * 16 + (lane & 15), col = xg * 64 + (li & 7) * 8 + (li >> 3);
    gu64* yg = G + G_Y + (xg * YROWS) * 64 + li;
    const float xo = bf_lo(__builtin_amdgcn_raw_buffer_load_b16(rx, col * 2, 0, 0));
    WAIT_UNTIL(tstart + T_FSUM);
    unsigned fv[2], fw[1];
    sweep<2, 32>(yg + (H + hg) * 64, 256, tag, fv, tstart);
    sweep<1, 32>(yg + (H + 8 + (hg == 3 ? 0 : hg)) * 64, 0, tag, fw, tstart);
    const float fs = rbf(xrow_sum((asf(fv[0]) + asf(fv[1])) + (hg == 3 ? 0.f : asf(fw[0]))));
    LDS_BARRIER();
    while (lw[4] == 0 && !expired(tstart)) __builtin_amdgcn_s_sleep(1);
    __builtin_amdgcn_s_sleep(SLEEP_Y);
    unsigned yv[8];
    sweep<8, 1>(yg + hg * 64, 256, tag, yv, tstart);
    float a = 0.f;
#pragma unroll
    for (int i = 0; i < 8; ++i) a += asf(yv[i]);
    a = rbf(xrow_sum(a));
    const float o = rbf(xo + a) + fs;
    if (lane < 16) out[col] = (uint16_t)pk_bf16(o, o);
  }
}

}

extern "C" void kernel_launch(void* const* d_in, const int* in_sizes, int n_in, void* d_out, int out_size, void* d_ws, size_t ws_size,
                              hipStream_t stream) {
  fused_decode<<<NWG, NTHR, 0, stream>>>((const uint16_t*)d_in[0], (const char*)d_in[1], (const char*)d_in[2], (const char*)d_in[3],
                                         (const char*)d_in[4], (const char*)d_in[6], (const char*)d_in[7], (const char*)d_in[8],
                                         (char*)d_ws, (uint16_t*)d_out);
}
```
